```python
import torch

def _softmax(x, axis=-1):
    return torch.softmax(x, dim=axis)
def _gelu(x):
    return torch.nn.functional.gelu(x, approximate="tanh")
def _silu(x):
    return torch.nn.functional.silu(x)
def _sigmoid(x):
    return torch.sigmoid(x)
def _layernorm(x, axis=-1, eps=1e-5):
    m = x.mean(dim=axis, keepdim=True)
    v = x.var(dim=axis, keepdim=True, unbiased=False)
    return (x - m) / torch.sqrt(v + eps)
def _scatter_add(tgt, idx, src, axis=0):
    return tgt.clone().index_add_(axis, idx, src)
def _segment_sum(src, seg, n):
    out = torch.zeros((n,) + src.shape[1:], dtype=src.dtype)
    return out.index_add_(0, seg, src)
def _pad(x, pw):
    flat = tuple(v for pair in reversed(pw) for v in pair)
    return torch.nn.functional.pad(x, flat)

kernel_name = 'recurrent_scan__compressed_kv_cla__moe_arch_mod__decode'

def setup_inputs():

    _w = (lambda a: (lambda t: t.to(torch.bfloat16) if t.is_floating_point() else t)(torch.as_tensor(a)))
    def _A_setup():
        B, H, Sq, Sk, D, M = 4, 32, 1, 4096, 128, 512
        Dc = 4 * D
        return {
            "x": _w((lambda *s: torch.randn(*s, dtype=torch.bfloat16) * 0.05)(B, Sq, M)),
            "c_kv": _w((lambda *s: torch.randn(*s, dtype=torch.bfloat16) * 0.8)(B, Sk, Dc)),
            "w_q": _w((lambda *s: torch.randn(*s, dtype=torch.bfloat16) * 1.6)(M, H, D) * (M ** -0.5)),
            "w_uk": _w((lambda *s: torch.randn(*s, dtype=torch.bfloat16) * 1.6)(Dc, H, D) * (Dc ** -0.5)),
            "w_uv": _w((lambda *s: torch.randn(*s, dtype=torch.bfloat16) * 0.8)(Dc, H, D) * (Dc ** -0.5)),
            "w_o": _w((lambda *s: torch.randn(*s, dtype=torch.bfloat16) * 0.8)(H, D, M) * ((H * D) ** -0.5)),
        }
    _A_in = _A_setup()

    def _B_setup():
        B, S, D, F, C = 4, 1, 512, 1792, 1
        return {
            "x": _w((lambda *s: torch.randn(*s, dtype=torch.bfloat16) * 0.05)(B, S, D)),
            "w_r": _w((lambda *s: torch.randn(*s, dtype=torch.bfloat16) * 0.8)(D)),
            "w_gate": _w((lambda *s: torch.randn(*s, dtype=torch.bfloat16) * 0.2)(D, F)),
            "w_up": _w((lambda *s: torch.randn(*s, dtype=torch.bfloat16) * 0.2)(D, F)),
            "w_down": _w((lambda *s: torch.randn(*s, dtype=torch.bfloat16) * 0.2)(F, D)),
        }
    _B_in = _B_setup()

    return {
        "A_x": _A_in["x"],
        "A_c_kv": _A_in["c_kv"],
        "A_w_q": _A_in["w_q"],
        "A_w_uk": _A_in["w_uk"],
        "A_w_uv": _A_in["w_uv"],
        "A_w_o": _A_in["w_o"],
        "B_x": _B_in["x"],
        "B_w_r": _B_in["w_r"],
        "B_w_gate": _B_in["w_gate"],
        "B_w_up": _B_in["w_up"],
        "B_w_down": _B_in["w_down"],
    }

def reference(A_x, A_c_kv, A_w_q, A_w_uk, A_w_uv, A_w_o, B_x, B_w_r, B_w_gate, B_w_up, B_w_down):
    def _A_ref(x, c_kv, w_q, w_uk, w_uv, w_o):
        B, Sq, M = x.shape[0], x.shape[1], x.shape[2]
        Sk = c_kv.shape[1]
        H, D = w_q.shape[1], w_q.shape[2]
        q = torch.einsum("bqm,mhd->bhqd", x, w_q)
        k = torch.einsum("bkc,chd->bhkd", c_kv, w_uk)
        v = torch.einsum("bkc,chd->bhkd", c_kv, w_uv)
        scores = torch.einsum("bhqd,bhkd->bhqk", q, k) * (D ** -0.5)
        off = Sk - Sq
        qi = torch.arange(Sq)[:, None]
        kj = torch.arange(Sk)[None, :]
        cmask = kj <= (qi + off)
        scores = torch.where(cmask[None, None], scores, -1e9)
        p = _softmax(scores, axis=-1)
        ctx = torch.einsum("bhqk,bhkd->bhqd", p, v)
        return torch.einsum("bhqd,hdm->bqm", ctx, w_o)

    def _B_ref(x, w_r, w_gate, w_up, w_down):
        B, S, D = x.shape
        C = 1
        r = x @ w_r
        idx = torch.argsort(-r, axis=-1)[:, :C]
        idx_e = (lambda x, n, axis: torch.repeat_interleave(x, n, dim=axis))(idx.reshape(B, C, 1), D, axis=2)
        x_sel = (lambda x, i, axis: torch.take_along_dim(x, i, dim=axis))(x, idx_e, axis=1)
        g = _silu(x_sel @ w_gate)
        u = x_sel @ w_up
        y = (g * u) @ w_down
        r_sel = _sigmoid((lambda x, i, axis: torch.take_along_dim(x, i, dim=axis))(r, idx, axis=1))
        y = y * r_sel.reshape(B, C, 1)
        b_off = (torch.arange(B) * S).reshape(B, 1)
        flat_idx = (idx + b_off).reshape(B * C)
        zero = (x * 0.0).reshape(B * S, D)
        upd = _scatter_add(zero, flat_idx, y.reshape(B * C, D), axis=0)
        return x + upd.reshape(B, S, D)

    x = A_x
    xn = _layernorm(x, axis=-1)
    s = _A_ref(xn, A_c_kv, A_w_q, A_w_uk, A_w_uv, A_w_o)
    g = _silu(_B_ref(xn, B_w_r, B_w_gate, B_w_up, B_w_down))
    return x + s * g
```

```cpp
#include <hip/hip_runtime.h>
#include <hip/hip_bf16.h>
#include <stdint.h>

typedef unsigned short u16;
typedef unsigned int u32;

static __device__ __forceinline__ float bf2f(u16 v) { return __uint_as_float(((u32)v) << 16); }
static __device__ __forceinline__ float rbf(float f) {
  u32 u = __float_as_uint(f);
  u += 0x7fffu + ((u >> 16) & 1u);
  return __uint_as_float(u & 0xffff0000u);
}
static __device__ __forceinline__ u16 f2bf(float f) {
  u32 u = __float_as_uint(f);
  u += 0x7fffu + ((u >> 16) & 1u);
  return (u16)(u >> 16);
}

constexpr int B = 4, H = 32, SK = 4096, D = 128, M = 512, DC = 512, F = 1792;
constexpr int NSPLIT = 64, KPS = SK / NSPLIT;

static __device__ void ln_rows(const u16* x, float* xn, float* red) {
  const int tid = threadIdx.x, wave = tid >> 6, lane = tid & 63;
  if (wave < 4) {
    float v[8];
    float s = 0.f;
    for (int i = 0; i < 8; ++i) { v[i] = bf2f(x[wave * M + lane + 64 * i]); s += v[i]; }
    for (int o = 32; o > 0; o >>= 1) s += __shfl_xor(s, o);
    const float mean = s * (1.0f / M);
    float q = 0.f;
    for (int i = 0; i < 8; ++i) { float d = v[i] - mean; q += d * d; }
    for (int o = 32; o > 0; o >>= 1) q += __shfl_xor(q, o);
    const float var = rbf(q * (1.0f / M));
    const float mb = rbf(mean);
    const float den = rbf(sqrtf(rbf(var + 1e-5f)));
    for (int i = 0; i < 8; ++i) xn[wave * M + lane + 64 * i] = rbf(rbf(v[i] - mb) / den);
  }
  __syncthreads();
}

__global__ __launch_bounds__(256) void k1(const u16* x, const u16* wq, const u16* wg, const u16* wu,
                                          float* q, float* hbuf) {
  __shared__ float xn[4 * M];
  __shared__ float red[4][4][128];
  ln_rows(x, xn, nullptr);
  const int tid = threadIdx.x, wave = tid >> 6, lane = tid & 63;
  const int blk = blockIdx.x;
  if (blk < 32) {
    float a0[4] = {0, 0, 0, 0}, a1[4] = {0, 0, 0, 0};
    for (int m = wave * 128; m < wave * 128 + 128; ++m) {
      u32 w = *(const u32*)(wq + (size_t)m * 4096 + blk * 128 + 2 * lane);
      float w0 = __uint_as_float(w << 16), w1 = __uint_as_float(w & 0xffff0000u);
      for (int b = 0; b < 4; ++b) { a0[b] += xn[b * M + m] * w0; a1[b] += xn[b * M + m] * w1; }
    }
    for (int b = 0; b < 4; ++b) { red[wave][b][2 * lane] = a0[b]; red[wave][b][2 * lane + 1] = a1[b]; }
    __syncthreads();
    for (int i = tid; i < 512; i += 256) {
      int b = i >> 7, d = i & 127;
      float s = red[0][b][d] + red[1][b][d] + red[2][b][d] + red[3][b][d];
      q[(b * H + blk) * D + d] = rbf(s);
    }
  } else {
    const int fb = blk - 32;
    float ag[4] = {0, 0, 0, 0}, au[4] = {0, 0, 0, 0};
    for (int m = wave * 128; m < wave * 128 + 128; ++m) {
      float g = bf2f(wg[(size_t)m * F + fb * 64 + lane]);
      float u = bf2f(wu[(size_t)m * F + fb * 64 + lane]);
      for (int b = 0; b < 4; ++b) { ag[b] += xn[b * M + m] * g; au[b] += xn[b * M + m] * u; }
    }
    for (int b = 0; b < 4; ++b) { red[wave][b][lane] = ag[b]; red[wave][b][64 + lane] = au[b]; }
    __syncthreads();
    {
      int b = tid >> 6, c = tid & 63;
      float g = rbf(red[0][b][c] + red[1][b][c] + red[2][b][c] + red[3][b][c]);
      float u = rbf(red[0][b][64 + c] + red[1][b][64 + c] + red[2][b][64 + c] + red[3][b][64 + c]);
      float sg = rbf(g / (1.0f + __expf(-g)));
      hbuf[b * F + fb * 64 + c] = rbf(sg * u);
    }
  }
}

__global__ __launch_bounds__(256) void k2(const float* q, const u16* wuk, const float* hbuf, const u16* wd,
                                          float* qabs, float* ypart) {
  const int tid = threadIdx.x, wave = tid >> 6, lane = tid & 63;
  const int blk = blockIdx.x;
  if (blk < 256) {
    const int h = blk >> 3, j = blk & 7;
    float qv[4][2];
    for (int b = 0; b < 4; ++b) { qv[b][0] = q[(b * H + h) * D + 2 * lane]; qv[b][1] = q[(b * H + h) * D + 2 * lane + 1]; }
    for (int c = j * 64 + wave * 16; c < j * 64 + wave * 16 + 16; ++c) {
      u32 w = *(const u32*)(wuk + (size_t)c * 4096 + h * 128 + 2 * lane);
      float w0 = __uint_as_float(w << 16), w1 = __uint_as_float(w & 0xffff0000u);
      for (int b = 0; b < 4; ++b) {
        float s = qv[b][0] * w0 + qv[b][1] * w1;
        for (int o = 32; o > 0; o >>= 1) s += __shfl_xor(s, o);
        if (lane == 0) qabs[(b * H + h) * DC + c] = s * 0.08838834764831845f;
      }
    }
  } else {
    const int fb = blk - 256;
    float a0[4] = {0, 0, 0, 0}, a1[4] = {0, 0, 0, 0};
    for (int f = fb * 64; f < fb * 64 + 64; ++f) {
      u32 w = *(const u32*)(wd + (size_t)f * M + 2 * tid);
      float w0 = __uint_as_float(w << 16), w1 = __uint_as_float(w & 0xffff0000u);
      for (int b = 0; b < 4; ++b) { float hv = hbuf[b * F + f]; a0[b] += hv * w0; a1[b] += hv * w1; }
    }
    for (int b = 0; b < 4; ++b) { ypart[(fb * 4 + b) * M + 2 * tid] = a0[b]; ypart[(fb * 4 + b) * M + 2 * tid + 1] = a1[b]; }
  }
}

__global__ __launch_bounds__(256) void k3(const float* qabs, const u16* ckv, float* opart, float* mpart, float* lpart) {
  __shared__ u16 kv[KPS][DC + 8];
  __shared__ float sc[H][KPS];
  const int tid = threadIdx.x, wave = tid >> 6, lane = tid & 63;
  const int b = blockIdx.x / NSPLIT, sp = blockIdx.x % NSPLIT;
  const u16* src = ckv + ((size_t)b * SK + sp * KPS) * DC;
  for (int i = tid; i < KPS * DC / 8; i += 256) {
    int r = i / (DC / 8), c8 = i % (DC / 8);
    *(uint4*)(&kv[r][c8 * 8]) = *(const uint4*)(src + (size_t)r * DC + c8 * 8);
  }
  __syncthreads();
  for (int hh = 0; hh < 8; ++hh) {
    const int h = wave * 8 + hh;
    const float* qa = qabs + (b * H + h) * DC;
    float s = 0.f;
    for (int c = 0; c < DC; ++c) s += qa[c] * bf2f(kv[lane][c]);
    float mx = s;
    for (int o = 32; o > 0; o >>= 1) mx = fmaxf(mx, __shfl_xor(mx, o));
    float p = __expf(s - mx);
    float l = p;
    for (int o = 32; o > 0; o >>= 1) l += __shfl_xor(l, o);
    sc[h][lane] = p;
    if (lane == 0) { mpart[(b * H + h) * NSPLIT + sp] = mx; lpart[(b * H + h) * NSPLIT + sp] = l; }
  }
  __syncthreads();
  for (int h = 0; h < H; ++h) {
    float a0 = 0.f, a1 = 0.f;
    for (int k = 0; k < KPS; ++k) {
      u32 w = *(const u32*)(&kv[k][2 * tid]);
      float p = sc[h][k];
      a0 += p * __uint_as_float(w << 16);
      a1 += p * __uint_as_float(w & 0xffff0000u);
    }
    float* o = opart + (((size_t)(b * H + h)) * NSPLIT + sp) * DC;
    o[2 * tid] = a0; o[2 * tid + 1] = a1;
  }
}

__global__ __launch_bounds__(256) void k4(const float* opart, const float* mpart, const float* lpart, const u16* wuv, float* ctx) {
  __shared__ float w[NSPLIT];
  __shared__ float oc[DC];
  __shared__ float red[4][128];
  const int tid = threadIdx.x, wave = tid >> 6, lane = tid & 63;
  const int bh = blockIdx.x, h = bh % H;
  if (wave == 0) {
    float m = mpart[bh * NSPLIT + lane], l = lpart[bh * NSPLIT + lane];
    float mx = m;
    for (int o = 32; o > 0; o >>= 1) mx = fmaxf(mx, __shfl_xor(mx, o));
    float e = __expf(m - mx);
    float L = e * l;
    for (int o = 32; o > 0; o >>= 1) L += __shfl_xor(L, o);
    w[lane] = e / L;
  }
  __syncthreads();
  for (int c = tid; c < DC; c += 256) {
    float s = 0.f;
    for (int sp = 0; sp < NSPLIT; ++sp) s += w[sp] * opart[((size_t)bh * NSPLIT + sp) * DC + c];
    oc[c] = s;
  }
  __syncthreads();
  float a0 = 0.f, a1 = 0.f;
  for (int c = wave * 128; c < wave * 128 + 128; ++c) {
    u32 ww = *(const u32*)(wuv + (size_t)c * 4096 + h * 128 + 2 * lane);
    a0 += oc[c] * __uint_as_float(ww << 16);
    a1 += oc[c] * __uint_as_float(ww & 0xffff0000u);
  }
  red[wave][2 * lane] = a0; red[wave][2 * lane + 1] = a1;
  __syncthreads();
  if (tid < 128) ctx[bh * D + tid] = rbf(red[0][tid] + red[1][tid] + red[2][tid] + red[3][tid]);
}

__global__ __launch_bounds__(256) void k5(const float* ctx, const u16* wo, float* spart) {
  const int tid = threadIdx.x, blk = blockIdx.x;
  float a0[4] = {0, 0, 0, 0}, a1[4] = {0, 0, 0, 0};
  for (int r = blk * 64; r < blk * 64 + 64; ++r) {
    u32 w = *(const u32*)(wo + (size_t)r * M + 2 * tid);
    float w0 = __uint_as_float(w << 16), w1 = __uint_as_float(w & 0xffff0000u);
    for (int b = 0; b < 4; ++b) { float c = ctx[b * 4096 + r]; a0[b] += c * w0; a1[b] += c * w1; }
  }
  for (int b = 0; b < 4; ++b) { spart[(blk * 4 + b) * M + 2 * tid] = a0[b]; spart[(blk * 4 + b) * M + 2 * tid + 1] = a1[b]; }
}

__global__ __launch_bounds__(512) void k6(const u16* x, const u16* wr, const float* spart, const float* ypart, u16* out) {
  __shared__ float xn[4 * M];
  __shared__ float red[8];
  ln_rows(x, xn, nullptr);
  const int tid = threadIdx.x, b = blockIdx.x, wave = tid >> 6, lane = tid & 63;
  float r = xn[b * M + tid] * bf2f(wr[tid]);
  for (int o = 32; o > 0; o >>= 1) r += __shfl_xor(r, o);
  if (lane == 0) red[wave] = r;
  __syncthreads();
  float rr = 0.f;
  for (int i = 0; i < 8; ++i) rr += red[i];
  rr = rbf(rr);
  const float rs = rbf(1.0f / (1.0f + __expf(-rr)));
  float s = 0.f, y = 0.f;
  for (int i = 0; i < 64; ++i) s += spart[(i * 4 + b) * M + tid];
  for (int i = 0; i < 28; ++i) y += ypart[(i * 4 + b) * M + tid];
  s = rbf(s);
  y = rbf(rbf(y) * rs);
  float z = rbf(xn[b * M + tid] + y);
  float g = rbf(z / (1.0f + __expf(-z)));
  out[b * M + tid] = f2bf(bf2f(x[b * M + tid]) + rbf(s * g));
}

extern "C" void kernel_launch(void* const* d_in, const int* in_sizes, int n_in,
                              void* d_out, int out_size, void* d_ws, size_t ws_size,
                              hipStream_t stream) {
  const u16* x = (const u16*)d_in[0];
  const u16* ckv = (const u16*)d_in[1];
  const u16* wq = (const u16*)d_in[2];
  const u16* wuk = (const u16*)d_in[3];
  const u16* wuv = (const u16*)d_in[4];
  const u16* wo = (const u16*)d_in[5];
  const u16* wr = (const u16*)d_in[7];
  const u16* wg = (const u16*)d_in[8];
  const u16* wu = (const u16*)d_in[9];
  const u16* wd = (const u16*)d_in[10];
  float* ws = (float*)d_ws;
  float* q = ws;
  float* hbuf = q + 16384;
  float* qabs = hbuf + 7168;
  float* ypart = qabs + 65536;
  float* mpart = ypart + 57344;
  float* lpart = mpart + 8192;
  float* ctx = lpart + 8192;
  float* spart = ctx + 16384;
  float* opart = spart + 131072;
  k1<<<32 + 28, 256, 0, stream>>>(x, wq, wg, wu, q, hbuf);
  k2<<<256 + 28, 256, 0, stream>>>(q, wuk, hbuf, wd, qabs, ypart);
  k3<<<B * NSPLIT, 256, 0, stream>>>(qabs, ckv, opart, mpart, lpart);
  k4<<<B * H, 256, 0, stream>>>(opart, mpart, lpart, wuv, ctx);
  k5<<<64, 256, 0, stream>>>(ctx, wo, spart);
  k6<<<4, 512, 0, stream>>>(x, wr, spart, ypart, (u16*)d_out);
}
```

```cpp
#include <hip/hip_runtime.h>
#include <stdint.h>

typedef unsigned short u16;
typedef unsigned int u32;
typedef unsigned long long u64;
typedef u32 v4u __attribute__((ext_vector_type(4)));
typedef u32 v2u __attribute__((ext_vector_type(2)));
typedef float f4 __attribute__((ext_vector_type(4)));
typedef short bf16x8 __attribute__((ext_vector_type(8)));
typedef short s16x4 __attribute__((ext_vector_type(4)));
typedef __bf16 bf16x2 __attribute__((ext_vector_type(2)));
#define LAS __attribute__((address_space(3)))
#define GAS __attribute__((address_space(1)))

#define STAMPS 0
#define NOCLEAR 0

constexpr int NWG = 256;
constexpr u32 WS_EPOCH = 0;
constexpr u32 WS_QA = 4096;
constexpr u32 WS_IN3 = WS_QA + 4 * 32 * 128 * 16;
constexpr u32 WS_ST3 = WS_IN3 + 32 * 8 * 16384;
constexpr u32 WS_IN5 = WS_ST3 + 32 * 128 * 4;
constexpr u32 WS_INY = WS_IN5 + 256 * 8192;
constexpr u32 WS_END = WS_INY + 256 * 7168;
constexpr int L_KV = 0;
constexpr int L_Q = 131072;
constexpr int L_XN = L_Q + 16384;
constexpr int L_P = L_XN + 4096;
constexpr int L_M = L_P + 4096;
constexpr int M_QS = L_M, M_WMAX = L_M + 1024, M_WSUM = L_M + 1536, M_RS = L_M + 2048, M_OCS = L_M + 2112,
              M_CTX = L_M + 2624, M_FRED = L_M + 3648, M_CNT = L_M + 4672, M_FIN = L_M + 4800;
constexpr int LDS_BYTES = L_M + 5120;
enum { C_XN = 0, C_RED = 1, C_QS = 2, C_CLR = 3, C_QRDY = 4, C_FFN = 5, C_KV0 = 8   };
constexpr u32 SENT = 0xFFFFFFFFu;
constexpr u32 SPIN_CAP = 1u << 16;

__device__ __forceinline__ float bf2f(u32 v) { return __uint_as_float(v << 16); }
__device__ __forceinline__ float lo_f(u32 v) { return __uint_as_float(v << 16); }
__device__ __forceinline__ float hi_f(u32 v) { return __uint_as_float(v & 0xffff0000u); }
__device__ __forceinline__ u32 pk_bf16(float a, float b) {
  bf16x2 r; r[0] = (__bf16)a; r[1] = (__bf16)b; return __builtin_bit_cast(u32, r);
}
__device__ __forceinline__ float rbf(float a) { return (float)((__bf16)a); }
__device__ __forceinline__ f4 mfma16(v4u a, v4u b, f4 c) {
  return __builtin_amdgcn_mfma_f32_16x16x32_bf16(__builtin_bit_cast(bf16x8, a), __builtin_bit_cast(bf16x8, b), c, 0, 0, 0);
}
__device__ __forceinline__ float dot2(u32 a, u32 b, float c) {
  return __builtin_amdgcn_fdot2_f32_bf16(__builtin_bit_cast(bf16x2, a), __builtin_bit_cast(bf16x2, b), c, false);
}
__device__ __forceinline__ u32 swz(u32 row) { return ((row & 3u) << 2) | ((row >> 2) & 3u); }
__device__ __forceinline__ v2u trread(LAS unsigned char* p) {
  return __builtin_bit_cast(v2u, __builtin_amdgcn_ds_read_tr16_b64_v4i16((LAS s16x4*)p));
}
__device__ __forceinline__ void glds16s(const void* sbase, u32 voff, u32 lds_dst) {
  u32 keep;
  asm volatile("s_mov_b32 %0, m0\n\ts_mov_b32 m0, %3\n\ts_nop 0\n\tglobal_load_lds_dwordx4 %1, %2\n\ts_mov_b32 m0, %0"
               : "=&s"(keep) : "v"(voff), "s"(sbase), "s"(lds_dst) : "memory");
}
template <int CTRL> __device__ __forceinline__ float dpp(float v) {
  return __int_as_float(__builtin_amdgcn_update_dpp(0, __float_as_int(v), CTRL, 0xf, 0xf, true));
}
__device__ __forceinline__ float sum16(float v) { v += dpp<0xB1>(v); v += dpp<0x4E>(v); v += dpp<0x141>(v); v += dpp<0x140>(v); return v; }
__device__ __forceinline__ float max16(float v) { v = fmaxf(v, dpp<0xB1>(v)); v = fmaxf(v, dpp<0x4E>(v)); v = fmaxf(v, dpp<0x141>(v)); v = fmaxf(v, dpp<0x140>(v)); return v; }
__device__ __forceinline__ float sumx16(float v) { auto r = __builtin_amdgcn_permlane16_swap(__float_as_uint(v), __float_as_uint(v), false, false); return __uint_as_float(r[0]) + __uint_as_float(r[1]); }
__device__ __forceinline__ float sumx32(float v) { auto r = __builtin_amdgcn_permlane32_swap(__float_as_uint(v), __float_as_uint(v), false, false); return __uint_as_float(r[0]) + __uint_as_float(r[1]); }
__device__ __forceinline__ float maxx16(float v) { auto r = __builtin_amdgcn_permlane16_swap(__float_as_uint(v), __float_as_uint(v), false, false); return fmaxf(__uint_as_float(r[0]), __uint_as_float(r[1])); }
__device__ __forceinline__ float maxx32(float v) { auto r = __builtin_amdgcn_permlane32_swap(__float_as_uint(v), __float_as_uint(v), false, false); return fmaxf(__uint_as_float(r[0]), __uint_as_float(r[1])); }
__device__ __forceinline__ float sum64(float v) { return sumx32(sumx16(sum16(v))); }
__device__ __forceinline__ float tred32(float a, float b) { auto r = __builtin_amdgcn_permlane32_swap(__float_as_uint(a), __float_as_uint(b), false, false); return __uint_as_float(r[0]) + __uint_as_float(r[1]); }
__device__ __forceinline__ float tred16(float a, float b) { auto r = __builtin_amdgcn_permlane16_swap(__float_as_uint(a), __float_as_uint(b), false, false); return __uint_as_float(r[0]) + __uint_as_float(r[1]); }

#define BAR() do { asm volatile("s_waitcnt lgkmcnt(0)" ::: "memory"); __builtin_amdgcn_s_barrier(); asm volatile("" ::: "memory"); } while (0)
#define CFENCE() asm volatile("" ::: "memory")
__device__ __forceinline__ u32 umax4(v4u v) { u32 a = v.x > v.y ? v.x : v.y, b = v.z > v.w ? v.z : v.w; return a > b ? a : b; }
__device__ __forceinline__ v4u nosent(v4u v) {
  v.x = v.x < 0xFFFFFFFEu ? v.x : 0xFFFFFFFEu; v.y = v.y < 0xFFFFFFFEu ? v.y : 0xFFFFFFFEu;
  v.z = v.z < 0xFFFFFFFEu ? v.z : 0xFFFFFFFEu; v.w = v.w < 0xFFFFFFFEu ? v.w : 0xFFFFFFFEu; return v;
}
__device__ __forceinline__ void lds_signal(LAS unsigned char* L, int c, int lane) {
  asm volatile("s_waitcnt lgkmcnt(0)" ::: "memory");
  if (lane == 0) __hip_atomic_fetch_add((LAS u32*)(L + M_CNT) + c, 1u, __ATOMIC_RELAXED, __HIP_MEMORY_SCOPE_WORKGROUP);
}
__device__ __forceinline__ void lds_wait(LAS unsigned char* L, int c, u32 target) {
  for (u32 spins = 0; spins < (1u << 24); ++spins) {
    if (*((volatile LAS u32*)(L + M_CNT) + c) >= target) break;
  }
  CFENCE();
}

__global__ __launch_bounds__(512, 2) void mega(const u16* __restrict__ x, const u16* __restrict__ ckv, const u16* __restrict__ wq,
                                               const u16* __restrict__ wuk, const u16* __restrict__ wuv, const u16* __restrict__ wo,
                                               const u16* __restrict__ wr, const u16* __restrict__ wg, const u16* __restrict__ wu,
                                               const u16* __restrict__ wd, u16* __restrict__ out, unsigned char* ws, u64* stamps) {
  extern __shared__ __attribute__((aligned(16))) unsigned char lds_raw[];
  LAS unsigned char* L = (LAS unsigned char*)lds_raw;
  const int tid = threadIdx.x, lane = tid & 63;
  const int wave = __builtin_amdgcn_readfirstlane(tid >> 6);
  const int g = blockIdx.x, xg = g & 7, idx = g >> 3;
  const int h = xg * 4 + (idx >> 3), j = idx & 7;
  const int hg = idx & 1, kb = xg * 16 + (idx >> 1), ab = kb >> 5, as = kb & 31;
  const int i16 = lane & 15, gq = lane >> 4;
  const __amdgpu_buffer_rsrc_t rs = __builtin_amdgcn_make_buffer_rsrc((void*)ws, 0, (int)WS_END, 0x00020000);
  const u32 lds_base = (u32)(size_t)lds_raw;
#define STAMP(k)
  STAMP(0);
  u32 tagA = 0, tagB = 0;
  if (tid < 16) *((LAS u32*)(L + M_CNT) + tid) = 0u;
  v4u w4[4], wdn[8];
  const int fg = xg * 28 + idx;

  if (wave < 4) {
    const int a = wave;
    const v4u xv = *(const GAS v4u*)(x + a * 512 + lane * 8);
    const v4u wrv = *(const GAS v4u*)(wr + lane * 8);
    const v2u ep = __builtin_amdgcn_raw_buffer_load_b64(rs, WS_EPOCH, 0, 16);
    v4u wqr[4][8], wukf[4];
    {
      const __amdgpu_buffer_rsrc_t rq = __builtin_amdgcn_make_buffer_rsrc((void*)wq, 0, 512 * 4096 * 2, 0x00020000);
      const u32 voff = ((128 * a + 8 * gq) * 4096 + h * 128 + 8 * i16) * 2;
#pragma unroll
      for (int t = 0; t < 4; ++t)
#pragma unroll
        for (int jj = 0; jj < 8; ++jj) wqr[t][jj] = __builtin_amdgcn_raw_buffer_load_b128(rq, voff, (32 * t + jj) * 8192, 0);
    }
    const u16* wukp = wuk + (size_t)(64 * j + 16 * a + i16) * 4096 + h * 128 + 8 * gq;
#pragma unroll
    for (int t = 0; t < 4; ++t) wukf[t] = *(const GAS v4u*)(wukp + 32 * t);
    __builtin_amdgcn_sched_barrier(0);
    BAR();
    STAMP(12);
    float xf[8] = {lo_f(xv.x), hi_f(xv.x), lo_f(xv.y), hi_f(xv.y), lo_f(xv.z), hi_f(xv.z), lo_f(xv.w), hi_f(xv.w)};
    float s = ((xf[0] + xf[1]) + (xf[2] + xf[3])) + ((xf[4] + xf[5]) + (xf[6] + xf[7]));
    const float mean = sum64(s) * (1.0f / 512.0f);
    float q2 = 0.f;
#pragma unroll
    for (int e = 0; e < 8; ++e) { const float d = xf[e] - mean; q2 += d * d; }
    const float var = rbf(sum64(q2) * (1.0f / 512.0f));
    const float mb = rbf(mean);
    const float den = rbf(sqrtf(rbf(var + 1e-5f)));
    float xn[8];
#pragma unroll
    for (int e = 0; e < 8; ++e) xn[e] = rbf(xf[e] - mb) / den;
    v4u xnp = {pk_bf16(xn[0], xn[1]), pk_bf16(xn[2], xn[3]), pk_bf16(xn[4], xn[5]), pk_bf16(xn[6], xn[7])};
    *(LAS v4u*)(L + L_XN + a * 1024 + lane * 16) = xnp;
    lds_signal(L, C_XN, lane);
    float r = dot2(xnp.x, wrv.x, 0.f); r = dot2(xnp.y, wrv.y, r); r = dot2(xnp.z, wrv.z, r); r = dot2(xnp.w, wrv.w, r);
    r = rbf(sum64(r));
    if (lane == 0) *(LAS float*)(L + M_RS + a * 4) = rbf(1.0f / (1.0f + __expf(-r)));
    tagA = ep.x + 1u; tagB = ep.y + 1u;
    lds_wait(L, C_XN, 4);
    STAMP(1);
    f4 acc[8];
#pragma unroll
    for (int cc = 0; cc < 8; ++cc) acc[cc] = f4{0.f, 0.f, 0.f, 0.f};
#pragma unroll
    for (int t = 0; t < 4; ++t) {
      const v4u af = *(const LAS v4u*)(L + L_XN + (i16 >> 2) * 1024 + (128 * a + 32 * t + 8 * gq) * 2);
#pragma unroll
      for (int cc = 0; cc < 8; ++cc) {
        const u32 sel = (cc & 1) ? 0x07060302u : 0x05040100u;
        v4u bfr;
        bfr.x = __builtin_amdgcn_perm(wqr[t][1][cc >> 1], wqr[t][0][cc >> 1], sel);
        bfr.y = __builtin_amdgcn_perm(wqr[t][3][cc >> 1], wqr[t][2][cc >> 1], sel);
        bfr.z = __builtin_amdgcn_perm(wqr[t][5][cc >> 1], wqr[t][4][cc >> 1], sel);
        bfr.w = __builtin_amdgcn_perm(wqr[t][7][cc >> 1], wqr[t][6][cc >> 1], sel);
        acc[cc] = mfma16(af, bfr, acc[cc]);
      }
    }
    LAS f4* red = (LAS f4*)(L + L_Q + ((a * 4 + gq) * 128 + 8 * i16) * 4);
    red[0] = f4{acc[0][0], acc[1][0], acc[2][0], acc[3][0]};
    red[1] = f4{acc[4][0], acc[5][0], acc[6][0], acc[7][0]};
    lds_signal(L, C_RED, lane);
    STAMP(2);
    lds_wait(L, C_RED, 4);
    {
      float s0 = 0.f, s1 = 0.f;
#pragma unroll
      for (int aa = 0; aa < 4; ++aa) {
        const v2u p = *(const LAS v2u*)(L + L_Q + ((aa * 4 + a) * 128 + lane * 2) * 4);
        s0 += __uint_as_float(p.x); s1 += __uint_as_float(p.y);
      }
      *(LAS u32*)(L + M_QS + (a * 128 + lane * 2) * 2) = pk_bf16(s0, s1);
    }
    lds_signal(L, C_QS, lane);
    lds_wait(L, C_QS, 4);
    f4 qa = {0.f, 0.f, 0.f, 0.f};
#pragma unroll
    for (int t = 0; t < 4; ++t) {
      const v4u qf = *(const LAS v4u*)(L + M_QS + ((i16 & 3) * 128 + 32 * t + 8 * gq) * 2);
      qa = mfma16(wukf[t], qf, qa);
    }
    const float SC = 0.08838834764831845f * 1.4426950408889634f;
    lds_wait(L, C_CLR, 4);
    if (i16 < 4) {
      v4u o = {pk_bf16(qa[0] * SC, qa[1] * SC), tagA, pk_bf16(qa[2] * SC, qa[3] * SC), tagB};
      __builtin_amdgcn_raw_buffer_store_b128(o, rs, WS_QA + ((i16 * 32 + h) * 128 + 16 * j + 4 * a + gq) * 16, 0, 16);
    }
    STAMP(3);
    {
      const u32 src = WS_QA + ((ab * 32 + 16 * hg) * 128 + tid) * 16;
      const u32 srcp = WS_QA + ((ab * 32 + 16 * (1 - hg) + ((tid & 127) >> 3)) * 128 + 16 * (tid & 7)) * 16;
      v4u v[8];
      for (u32 spins = 0; spins < SPIN_CAP; ++spins) {
        CFENCE();
#pragma unroll
        for (int u = 0; u < 8; ++u) v[u] = __builtin_amdgcn_raw_buffer_load_b128(rs, src, u * 4096, 16);
        const v4u pv = __builtin_amdgcn_raw_buffer_load_b128(rs, srcp, 0, 16);
        u32 bad = (pv.y ^ tagA) | (pv.w ^ tagB);
#pragma unroll
        for (int u = 0; u < 8; ++u) bad |= (v[u].y ^ tagA) | (v[u].w ^ tagB);
        if (__all(bad == 0u)) break;
        __builtin_amdgcn_s_sleep(1);
      }
      const int cu = tid & 127, hb = tid >> 7;
#pragma unroll
      for (int u = 0; u < 8; ++u) {
        const int hh = 2 * u + hb;
        *(LAS v2u*)(L + L_Q + hh * 1024 + (((cu >> 1) ^ swz(hh)) * 16) + (cu & 1) * 8) = v2u{v[u].x, v[u].z};
      }
    }
    lds_signal(L, C_QRDY, lane);
    STAMP(4);
  } else {
    const int tl = tid - 256, lw = wave - 4;
    BAR();
    const v4u sv = {SENT, SENT, SENT, SENT};
#pragma unroll
    for (int u = 0; u < 4; ++u) __builtin_amdgcn_raw_buffer_store_b128(sv, rs, WS_IN3 + (h * 8 + j) * 16384 + tl * 16, u * 4096, 16);
#pragma unroll
    for (int u = 0; u < 2; ++u) __builtin_amdgcn_raw_buffer_store_b128(sv, rs, WS_IN5 + g * 8192 + tl * 16, u * 4096, 16);
    __builtin_amdgcn_raw_buffer_store_b128(sv, rs, WS_INY + g * 7168 + tl * 16, 0, 16);
    __builtin_amdgcn_raw_buffer_store_b128(sv, rs, tl < 192 ? WS_INY + g * 7168 + (256 + tl) * 16 : WS_ST3 + h * 512 + j * 64 + (tl & 3) * 16, 0, 16);
    CFENCE();
    STAMP(1);
    const u16* kvp = ckv + ((size_t)ab * 4096 + as * 128 + lw * 32) * 512;
#pragma unroll 4
    for (int r = 0; r < 32; ++r) {
      glds16s(kvp + (size_t)r * 512, (u32)((lane ^ swz(r)) * 16), lds_base + L_KV + (lw * 32 + r) * 1024);
    }
    STAMP(2);
    asm volatile("s_waitcnt vmcnt(32)" ::: "memory");
    lds_signal(L, C_CLR, lane);
    STAMP(3);
  }
  lds_wait(L, C_QRDY, 4);
  STAMP(5);
  {
    const u32 sz = swz(i16);
    v4u qf[16];
    {
      LAS unsigned char* qrow = L + L_Q + i16 * 1024;
#pragma unroll
      for (int t = 0; t < 16; ++t) qf[t] = *(const LAS v4u*)(qrow + 256 * (t >> 2) + 16 * ((4 * (t & 3) + gq) ^ sz));
    }
    if (wave >= 4) {
      asm volatile("s_waitcnt vmcnt(16)" ::: "memory");
      lds_signal(L, C_KV0 + 2 * (wave - 4), lane);
      asm volatile("s_waitcnt vmcnt(0)" ::: "memory");
      lds_signal(L, C_KV0 + 2 * (wave - 4) + 1, lane);
      STAMP(4);
      if (idx < 28) {
        const int lw = wave - 4;
        const u16* wsrc = ((lw & 1) ? wu : wg) + (size_t)(256 * (lw >> 1) + 4 * lane) * 1792 + 8 * fg;
#pragma unroll
        for (int r = 0; r < 4; ++r) w4[r] = *(const GAS v4u*)(wsrc + (size_t)r * 1792);
      }
    }
    lds_wait(L, C_KV0 + wave, 1);
    f4 sacc = {0.f, 0.f, 0.f, 0.f};
    {
      LAS unsigned char* kvrow = L + L_KV + (16 * wave + i16) * 1024;
#pragma unroll
      for (int t = 0; t < 16; ++t) {
        const v4u af = *(const LAS v4u*)(kvrow + 256 * (t >> 2) + 16 * ((4 * (t & 3) + gq) ^ sz));
        sacc = mfma16(af, qf[t], sacc);
      }
    }
    float mx = fmaxf(fmaxf(sacc[0], sacc[1]), fmaxf(sacc[2], sacc[3]));
    mx = maxx32(maxx16(mx));
    if (lane < 16) *(LAS float*)(L + M_WMAX + (lane * 8 + wave) * 4) = mx;
    STAMP(13);
    const int q4 = i16 >> 2, p4 = i16 & 3;
    v4u vf[4][4];
#pragma unroll
    for (int t = 0; t < 4; ++t) {
      lds_wait(L, C_KV0 + 2 * t, 1);
      lds_wait(L, C_KV0 + 2 * t + 1, 1);
      const int k0 = 32 * t + 8 * gq + q4, k1 = k0 + 4;
#pragma unroll
      for (int tt = 0; tt < 4; ++tt) {
        const v2u lo = trread(L + L_KV + k0 * 1024 + (((8 * wave + 4 * (tt >> 1) + p4) ^ swz(k0)) * 16) + 8 * (tt & 1));
        const v2u hi = trread(L + L_KV + k1 * 1024 + (((8 * wave + 4 * (tt >> 1) + p4) ^ swz(k1)) * 16) + 8 * (tt & 1));
        vf[t][tt] = v4u{lo.x, lo.y, hi.x, hi.y};
      }
    }
    BAR();
    if (wave >= 4) {
      const int lw = wave - 4;
#pragma unroll
      for (int r = 0; r < 4; ++r) {
        const int row = (lw * 4 + r) * 4 + gq;
        glds16s(wuv + (size_t)(64 * j) * 4096 + h * 128, (u32)(row * 8192 + ((i16 ^ swz(row)) * 16)), lds_base + L_Q + (lw * 4 + r) * 1024);
      }
    }
    const f4 m0 = *(const LAS f4*)(L + M_WMAX + i16 * 32), m1 = *(const LAS f4*)(L + M_WMAX + i16 * 32 + 16);
    const float m = fmaxf(fmaxf(fmaxf(m0[0], m0[1]), fmaxf(m0[2], m0[3])), fmaxf(fmaxf(m1[0], m1[1]), fmaxf(m1[2], m1[3])));
    const u32 p01 = pk_bf16(__builtin_amdgcn_exp2f(sacc[0] - m), __builtin_amdgcn_exp2f(sacc[1] - m));
    const u32 p23 = pk_bf16(__builtin_amdgcn_exp2f(sacc[2] - m), __builtin_amdgcn_exp2f(sacc[3] - m));
    *(LAS v2u*)(L + L_P + i16 * 256 + (((2 * wave + (gq >> 1)) ^ sz) * 16) + (gq & 1) * 8) = v2u{p01, p23};
    float ls = (lo_f(p01) + hi_f(p01)) + (lo_f(p23) + hi_f(p23));
    ls = sumx32(sumx16(ls));
    if (lane < 16) *(LAS float*)(L + M_WSUM + (lane * 8 + wave) * 4) = ls;
    BAR();
    STAMP(14);
    f4 oacc[4];
#pragma unroll
    for (int tt = 0; tt < 4; ++tt) oacc[tt] = f4{0.f, 0.f, 0.f, 0.f};
#pragma unroll
    for (int t = 0; t < 4; ++t) {
      const v4u pf = *(const LAS v4u*)(L + L_P + i16 * 256 + (((4 * t + gq) ^ sz) * 16));
#pragma unroll
      for (int tt = 0; tt < 4; ++tt) oacc[tt] = mfma16(vf[t][tt], pf, oacc[tt]);
    }
    const f4 l0 = *(const LAS f4*)(L + M_WSUM + i16 * 32), l1 = *(const LAS f4*)(L + M_WSUM + i16 * 32 + 16);
    const float l = ((l0[0] + l0[1]) + (l0[2] + l0[3])) + ((l1[0] + l1[1]) + (l1[2] + l1[3]));
    const float il = __builtin_amdgcn_rcpf(l);
    const int head = 16 * hg + i16;
#pragma unroll
    for (int e = 0; e < 2; ++e) {
      v4u o = {pk_bf16(oacc[2 * e][0] * il, oacc[2 * e][1] * il), pk_bf16(oacc[2 * e][2] * il, oacc[2 * e][3] * il),
               pk_bf16(oacc[2 * e + 1][0] * il, oacc[2 * e + 1][1] * il), pk_bf16(oacc[2 * e + 1][2] * il, oacc[2 * e + 1][3] * il)};
      __builtin_amdgcn_raw_buffer_store_b128(nosent(o), rs, WS_IN3 + (((head * 8 + wave) * 4 + ab) * 32 + as) * 128 + e * 64 + gq * 16, 0, 16);
    }
    if (wave == 0 && lane < 16) {
      u32 lse = __float_as_uint(m + __builtin_amdgcn_logf(l));
      lse = lse < 0xFFFFFFFEu ? lse : 0xFFFFFFFEu;
      __builtin_amdgcn_raw_buffer_store_b32(lse, rs, WS_ST3 + ((head * 4 + ab) * 32 + as) * 4, 0, 16);
    }
  }
  STAMP(6);
  if (wave < 4) {
    const int a = wave, chunk = lane & 7, srow = lane >> 3;
    const u32 src = WS_IN3 + (h * 8 + j) * 16384 + (a * 32 + srow) * 128 + chunk * 16;
    const u32 srcs = WS_ST3 + ((h * 4 + a) * 32 + srow) * 4;
    v4u v[4]; u32 lb[4];
    for (u32 spins = 0; spins < SPIN_CAP; ++spins) {
      CFENCE();
#pragma unroll
      for (int u = 0; u < 4; ++u) v[u] = __builtin_amdgcn_raw_buffer_load_b128(rs, src + u * 1024, 0, 16);
#pragma unroll
      for (int u = 0; u < 4; ++u) lb[u] = __builtin_amdgcn_raw_buffer_load_b32(rs, srcs + u * 32, 0, 16);
      u32 mxv = umax4(v4u{lb[0], lb[1], lb[2], lb[3]});
#pragma unroll
      for (int u = 0; u < 4; ++u) { const u32 t = umax4(v[u]); mxv = mxv > t ? mxv : t; }
      if (__all(mxv != SENT)) break;
      __builtin_amdgcn_s_sleep(1);
    }
    STAMP(7);
    float M = fmaxf(fmaxf(__uint_as_float(lb[0]), __uint_as_float(lb[1])), fmaxf(__uint_as_float(lb[2]), __uint_as_float(lb[3])));
    M = fmaxf(M, dpp<0x128>(M));
    M = maxx32(maxx16(M));
    float ex[4];
#pragma unroll
    for (int u = 0; u < 4; ++u) ex[u] = __builtin_amdgcn_exp2f(__uint_as_float(lb[u]) - M);
    float Ls = (ex[0] + ex[1]) + (ex[2] + ex[3]);
    Ls += dpp<0x128>(Ls);
    Ls = sumx32(sumx16(Ls));
    float oc[8];
#pragma unroll
    for (int e = 0; e < 8; ++e) oc[e] = 0.f;
#pragma unroll
    for (int u = 0; u < 4; ++u) {
      const float wv = ex[u];
      oc[0] += wv * lo_f(v[u].x); oc[1] += wv * hi_f(v[u].x); oc[2] += wv * lo_f(v[u].y); oc[3] += wv * hi_f(v[u].y);
      oc[4] += wv * lo_f(v[u].z); oc[5] += wv * hi_f(v[u].z); oc[6] += wv * lo_f(v[u].w); oc[7] += wv * hi_f(v[u].w);
    }
    float r4[4], r2[2];
#pragma unroll
    for (int e = 0; e < 4; ++e) r4[e] = tred32(oc[e], oc[4 + e]);
#pragma unroll
    for (int e = 0; e < 2; ++e) r2[e] = tred16(r4[e], r4[2 + e]);
    r2[0] += dpp<0x128>(r2[0]); r2[1] += dpp<0x128>(r2[1]);
    const float iL = __builtin_amdgcn_rcpf(Ls);
    if ((lane & 8) == 0) *(LAS u32*)(L + M_OCS + a * 128 + chunk * 16 + gq * 4) = pk_bf16(r2[0] * iL, r2[1] * iL);
  } else {
    const int lw = wave - 4;
    const u16* wop = wo + ((size_t)h * 128 + lw * 32) * 512;
    if (idx < 28) {
#pragma unroll
      for (int f = 0; f < 8; ++f) wdn[f] = *(const GAS v4u*)(wd + (size_t)(8 * fg + f) * 512 + lane * 8);
      float acc[32];
#pragma unroll
      for (int b = 0; b < 4; ++b) {
        const v2u xp = *(const LAS v2u*)(L + L_XN + b * 1024 + (256 * (lw >> 1) + 4 * lane) * 2);
#pragma unroll
        for (int f = 0; f < 8; ++f) {
          const u32 sel = (f & 1) ? 0x07060302u : 0x05040100u;
          const u32 p01 = __builtin_amdgcn_perm(w4[1][f >> 1], w4[0][f >> 1], sel);
          const u32 p23 = __builtin_amdgcn_perm(w4[3][f >> 1], w4[2][f >> 1], sel);
          acc[b * 8 + f] = dot2(p23, xp.y, dot2(p01, xp.x, 0.f));
        }
      }
      STAMP(12);
      float n16[16], n8[8];
#pragma unroll
      for (int k = 0; k < 16; ++k) n16[k] = tred32(acc[k], acc[16 + k]);
#pragma unroll
      for (int k = 0; k < 8; ++k) n8[k] = sum16(tred16(n16[k], n16[8 + k]));
      if (i16 == 0) {
        *(LAS f4*)(L + M_FRED + (lw * 32 + gq * 8) * 4) = f4{n8[0], n8[1], n8[2], n8[3]};
        *(LAS f4*)(L + M_FRED + (lw * 32 + gq * 8 + 4) * 4) = f4{n8[4], n8[5], n8[6], n8[7]};
      }
      lds_signal(L, C_FFN, lane);
    }
#pragma unroll 4
    for (int r = 0; r < 32; ++r) glds16s(wop + (size_t)r * 512, (u32)((lane ^ swz(r)) * 16), lds_base + L_KV + (lw * 32 + r) * 1024);
    if (idx < 28) {
      lds_wait(L, C_FFN, 4);
      STAMP(13);
      const int f = lane & 7;
      const float gsum = rbf(*(const LAS float*)(L + M_FRED + (0 * 32 + lw * 8 + f) * 4) + *(const LAS float*)(L + M_FRED + (2 * 32 + lw * 8 + f) * 4));
      const float usum = rbf(*(const LAS float*)(L + M_FRED + (1 * 32 + lw * 8 + f) * 4) + *(const LAS float*)(L + M_FRED + (3 * 32 + lw * 8 + f) * 4));
      const float hv = rbf(rbf(gsum / (1.0f + __expf(-gsum))) * usum);
      float y[8];
#pragma unroll
      for (int e = 0; e < 8; ++e) y[e] = 0.f;
#pragma unroll
      for (int ff = 0; ff < 8; ++ff) {
        const float hh = __int_as_float(__builtin_amdgcn_readlane(__float_as_int(hv), ff));
        y[0] += hh * lo_f(wdn[ff].x); y[1] += hh * hi_f(wdn[ff].x); y[2] += hh * lo_f(wdn[ff].y); y[3] += hh * hi_f(wdn[ff].y);
        y[4] += hh * lo_f(wdn[ff].z); y[5] += hh * hi_f(wdn[ff].z); y[6] += hh * lo_f(wdn[ff].w); y[7] += hh * hi_f(wdn[ff].w);
      }
      const u32 dst = WS_INY + ((lw * 64 + lane) * 224 + fg) * 32;
      __builtin_amdgcn_raw_buffer_store_b128(nosent(__builtin_bit_cast(v4u, f4{y[0], y[1], y[2], y[3]})), rs, dst, 0, 16);
      __builtin_amdgcn_raw_buffer_store_b128(nosent(__builtin_bit_cast(v4u, f4{y[4], y[5], y[6], y[7]})), rs, dst + 16, 0, 16);
      STAMP(14);
    }
    asm volatile("s_waitcnt vmcnt(0)" ::: "memory");
    STAMP(7);
  }
  BAR();
  STAMP(8);
  {
    const int q4 = i16 >> 2, p4 = i16 & 3;
    f4 c = {0.f, 0.f, 0.f, 0.f};
#pragma unroll
    for (int t = 0; t < 2; ++t) {
      const v4u of = *(const LAS v4u*)(L + M_OCS + (i16 & 3) * 128 + (32 * t + 8 * gq) * 2);
      const int r0 = 32 * t + 8 * gq + q4, r1 = r0 + 4;
      const v2u lo = trread(L + L_Q + r0 * 256 + (((2 * wave + (p4 >> 1)) ^ swz(r0)) * 16) + 8 * (p4 & 1));
      const v2u hi = trread(L + L_Q + r1 * 256 + (((2 * wave + (p4 >> 1)) ^ swz(r1)) * 16) + 8 * (p4 & 1));
      c = mfma16(v4u{lo.x, lo.y, hi.x, hi.y}, of, c);
    }
    if (i16 < 4) *(LAS v2u*)(L + M_CTX + (i16 * 128 + 16 * wave + 4 * gq) * 2) = v2u{pk_bf16(c[0], c[1]), pk_bf16(c[2], c[3])};
    v4u wf[4][4];
#pragma unroll
    for (int t = 0; t < 4; ++t) {
      const int r0 = 32 * t + 8 * gq + q4, r1 = r0 + 4;
#pragma unroll
      for (int tt = 0; tt < 4; ++tt) {
        const v2u lo = trread(L + L_KV + r0 * 1024 + (((8 * wave + 2 * tt + (p4 >> 1)) ^ swz(r0)) * 16) + 8 * (p4 & 1));
        const v2u hi = trread(L + L_KV + r1 * 1024 + (((8 * wave + 2 * tt + (p4 >> 1)) ^ swz(r1)) * 16) + 8 * (p4 & 1));
        wf[t][tt] = v4u{lo.x, lo.y, hi.x, hi.y};
      }
    }
    BAR();
    f4 sacc[4];
#pragma unroll
    for (int tt = 0; tt < 4; ++tt) sacc[tt] = f4{0.f, 0.f, 0.f, 0.f};
#pragma unroll
    for (int t = 0; t < 4; ++t) {
      const v4u cf = *(const LAS v4u*)(L + M_CTX + ((i16 & 3) * 128 + 32 * t + 8 * gq) * 2);
#pragma unroll
      for (int tt = 0; tt < 4; ++tt) sacc[tt] = mfma16(wf[t][tt], cf, sacc[tt]);
    }
    if (i16 < 4) {
#pragma unroll
      for (int tt = 0; tt < 4; ++tt) {
        const int f = i16 * 64 + 8 * wave + 2 * tt + (gq >> 1);
        __builtin_amdgcn_raw_buffer_store_b128(nosent(__builtin_bit_cast(v4u, sacc[tt])), rs, WS_IN5 + (f * 256 + g) * 32 + (gq & 1) * 16, 0, 16);
      }
    }
  }
  STAMP(9);
  if (wave < 4) {
    const u32 s5 = WS_IN5 + g * 8192 + tid * 32;
    const u32 sy = WS_INY + g * 7168 + (tid < 224 ? tid : 0) * 32;
    v4u a0, a1, y0, y1;
    for (u32 spins = 0; spins < SPIN_CAP; ++spins) {
      CFENCE();
      a0 = __builtin_amdgcn_raw_buffer_load_b128(rs, s5, 0, 16);
      a1 = __builtin_amdgcn_raw_buffer_load_b128(rs, s5 + 16, 0, 16);
      y0 = __builtin_amdgcn_raw_buffer_load_b128(rs, sy, 0, 16);
      y1 = __builtin_amdgcn_raw_buffer_load_b128(rs, sy + 16, 0, 16);
      const u32 mxv = umax4(v4u{umax4(a0), umax4(a1), umax4(y0), umax4(y1)});
      if (__all(mxv != SENT)) break;
      __builtin_amdgcn_s_sleep(1);
    }
    STAMP(10);
    float v[16];
#pragma unroll
    for (int e = 0; e < 4; ++e) {
      v[e] = __uint_as_float(a0[e]); v[4 + e] = __uint_as_float(a1[e]);
      v[8 + e] = tid < 224 ? __uint_as_float(y0[e]) : 0.f; v[12 + e] = tid < 224 ? __uint_as_float(y1[e]) : 0.f;
    }
    float n8[8], n4[4];
#pragma unroll
    for (int e = 0; e < 8; ++e) n8[e] = tred32(v[e], v[8 + e]);
#pragma unroll
    for (int e = 0; e < 4; ++e) n4[e] = sum16(tred16(n8[e], n8[4 + e]));
    if (i16 == 0) *(LAS f4*)(L + M_FIN + (wave * 16 + 4 * gq) * 4) = f4{n4[0], n4[1], n4[2], n4[3]};
  }
  BAR();
  if (tid < 8) {
    const int b = g >> 6, m = (g & 63) * 8 + tid;
    float s = 0.f, y = 0.f;
#pragma unroll
    for (int w = 0; w < 4; ++w) { s += *(const LAS float*)(L + M_FIN + (w * 16 + tid) * 4); y += *(const LAS float*)(L + M_FIN + (w * 16 + 8 + tid) * 4); }
    const float rsv = *(const LAS float*)(L + M_RS + b * 4);
    const float xnv = bf2f(*(const LAS u16*)(L + L_XN + (b * 512 + m) * 2));
    s = rbf(s);
    y = rbf(rbf(y) * rsv);
    const float z = rbf(xnv + y);
    const float gt = rbf(z / (1.0f + __expf(-z)));
    const float o = bf2f(x[b * 512 + m]) + rbf(s * gt);
    out[b * 512 + m] = __builtin_bit_cast(u16, (__bf16)o);
  }
  if (g == 0 && tid == 0) {
    __builtin_amdgcn_raw_buffer_store_b64(v2u{tagA, tagB}, rs, WS_EPOCH, 0, 16);
  }
}

extern "C" void kernel_launch(void* const* d_in, const int* in_sizes, int n_in,
                              void* d_out, int out_size, void* d_ws, size_t ws_size,
                              hipStream_t stream) {
  (void)in_sizes; (void)n_in; (void)out_size;
  (void)hipFuncSetAttribute((const void*)mega, hipFuncAttributeMaxDynamicSharedMemorySize, LDS_BYTES);
  u64* st = (u64*)((char*)d_ws + ws_size - (1 << 20));
  mega<<<NWG, 512, LDS_BYTES, stream>>>((const u16*)d_in[0], (const u16*)d_in[1], (const u16*)d_in[2], (const u16*)d_in[3],
                                        (const u16*)d_in[4], (const u16*)d_in[5], (const u16*)d_in[7], (const u16*)d_in[8],
                                        (const u16*)d_in[9], (const u16*)d_in[10], (u16*)d_out, (unsigned char*)d_ws, st);
}
```

```cpp
#include <hip/hip_runtime.h>
#include <stdint.h>

typedef unsigned short u16;
typedef unsigned int u32;
typedef unsigned long long u64;
typedef u32 v4u __attribute__((ext_vector_type(4)));
typedef u32 v2u __attribute__((ext_vector_type(2)));
typedef float f4 __attribute__((ext_vector_type(4)));
typedef short bf16x8 __attribute__((ext_vector_type(8)));
typedef short s16x4 __attribute__((ext_vector_type(4)));
typedef __bf16 bf16x2 __attribute__((ext_vector_type(2)));
#define LAS __attribute__((address_space(3)))
#define GAS __attribute__((address_space(1)))

#define STAMPS 0

#define WODELAY 450
#define KVDELAY 0
constexpr int NWG = 256;
constexpr u32 WS_EPOCH = 0;
constexpr u32 WS_QX = 4096;
constexpr u32 WS_FX = WS_QX + 32 * 8192;
constexpr u32 WS_QA = WS_FX + 112 * 8192;
constexpr u32 WS_IN3 = WS_QA + 4 * 32 * 128 * 16;
constexpr u32 WS_ST3 = WS_IN3 + 32 * 4 * 32768;
constexpr u32 WS_IN5 = WS_ST3 + 32 * 128 * 4;
constexpr u32 WS_INY = WS_IN5 + 128 * 8192;
constexpr u32 WS_END = WS_INY + 128 * 7168;
constexpr int L_BIG = 0;
constexpr int L_XN = 131072;
constexpr int L_P = L_XN + 4096;
constexpr int L_M = L_P + 8192;
constexpr int M_RED = L_P;
constexpr int M_QS = L_M;
constexpr int M_LSE = L_M + 1024;
constexpr int M_WMAX = L_M + 2048;
constexpr int M_WSUM = L_M + 3072;
constexpr int M_OCS = L_M + 4096;
constexpr int M_CTX = L_M + 5120;
constexpr int M_H = L_M + 6144;
constexpr int M_FIN = L_M + 6400;
constexpr int M_RS = L_M + 7424;
constexpr int M_CNT = L_M + 7488;
constexpr int LDS_BYTES = L_M + 7680;
enum { C_XN = 0, C_T = 1, C_RED = 2, C_CLR = 3, C_KV = 4, C_A = 5, C_UV = 6, C_UVR = 7, C_F = 8, C_FR = 9, C_H = 10, C_T2 = 11, C_QS = 12, C_LSE = 13, C_FA = 14, C_PR = 15, C_KQ = 16   };
constexpr u32 SENT = 0xFFFFFFFFu;
constexpr u32 SPIN_CAP = 1u << 16;

__device__ __forceinline__ float bf2f(u32 v) { return __uint_as_float(v << 16); }
__device__ __forceinline__ float lo_f(u32 v) { return __uint_as_float(v << 16); }
__device__ __forceinline__ float hi_f(u32 v) { return __uint_as_float(v & 0xffff0000u); }
__device__ __forceinline__ u32 pk_bf16(float a, float b) {
  bf16x2 r; r[0] = (__bf16)a; r[1] = (__bf16)b; return __builtin_bit_cast(u32, r);
}
__device__ __forceinline__ float rbf(float a) { return (float)((__bf16)a); }
__device__ __forceinline__ f4 mfma16(v4u a, v4u b, f4 c) {
  return __builtin_amdgcn_mfma_f32_16x16x32_bf16(__builtin_bit_cast(bf16x8, a), __builtin_bit_cast(bf16x8, b), c, 0, 0, 0);
}
__device__ __forceinline__ float dot2(u32 a, u32 b, float c) {
  return __builtin_amdgcn_fdot2_f32_bf16(__builtin_bit_cast(bf16x2, a), __builtin_bit_cast(bf16x2, b), c, false);
}
__device__ __forceinline__ u32 swz(u32 row) { return ((((row >> 3) ^ (row >> 2)) & 1u) << 3) | ((row & 3u) << 1) | ((row >> 3) & 1u); }
__device__ __forceinline__ v2u trread(LAS unsigned char* p) {
  return __builtin_bit_cast(v2u, __builtin_amdgcn_ds_read_tr16_b64_v4i16((LAS s16x4*)p));
}
__device__ __forceinline__ void glds16s(const void* sbase, u32 voff, u32 lds_dst) {
  u32 keep;
  asm volatile("s_mov_b32 %0, m0\n\ts_mov_b32 m0, %3\n\ts_nop 0\n\tglobal_load_lds_dwordx4 %1, %2 nt\n\ts_mov_b32 m0, %0"
               : "=&s"(keep) : "v"(voff), "s"(sbase), "s"(lds_dst) : "memory");
}
__device__ __forceinline__ void glds16v(const void* gsrc, u32 lds_dst) {
  u32 keep;
  asm volatile("s_mov_b32 %0, m0\n\ts_mov_b32 m0, %2\n\ts_nop 0\n\tglobal_load_lds_dwordx4 %1, off nt\n\ts_mov_b32 m0, %0"
               : "=&s"(keep) : "v"(gsrc), "s"(lds_dst) : "memory");
}
__device__ __forceinline__ v4u ldnt(const void* p) { return __builtin_nontemporal_load((const GAS v4u*)p); }
template <int CTRL> __device__ __forceinline__ float dpp(float v) {
  return __int_as_float(__builtin_amdgcn_update_dpp(0, __float_as_int(v), CTRL, 0xf, 0xf, true));
}
__device__ __forceinline__ float sum16(float v) { v += dpp<0xB1>(v); v += dpp<0x4E>(v); v += dpp<0x141>(v); v += dpp<0x140>(v); return v; }
__device__ __forceinline__ float sumx16(float v) { auto r = __builtin_amdgcn_permlane16_swap(__float_as_uint(v), __float_as_uint(v), false, false); return __uint_as_float(r[0]) + __uint_as_float(r[1]); }
__device__ __forceinline__ float sumx32(float v) { auto r = __builtin_amdgcn_permlane32_swap(__float_as_uint(v), __float_as_uint(v), false, false); return __uint_as_float(r[0]) + __uint_as_float(r[1]); }
__device__ __forceinline__ float maxx16(float v) { auto r = __builtin_amdgcn_permlane16_swap(__float_as_uint(v), __float_as_uint(v), false, false); return fmaxf(__uint_as_float(r[0]), __uint_as_float(r[1])); }
__device__ __forceinline__ float maxx32(float v) { auto r = __builtin_amdgcn_permlane32_swap(__float_as_uint(v), __float_as_uint(v), false, false); return fmaxf(__uint_as_float(r[0]), __uint_as_float(r[1])); }
__device__ __forceinline__ float sum64(float v) { return sumx32(sumx16(sum16(v))); }
__device__ __forceinline__ float tred32(float a, float b) { auto r = __builtin_amdgcn_permlane32_swap(__float_as_uint(a), __float_as_uint(b), false, false); return __uint_as_float(r[0]) + __uint_as_float(r[1]); }
__device__ __forceinline__ float tred16(float a, float b) { auto r = __builtin_amdgcn_permlane16_swap(__float_as_uint(a), __float_as_uint(b), false, false); return __uint_as_float(r[0]) + __uint_as_float(r[1]); }

#define BAR() do { asm volatile("s_waitcnt lgkmcnt(0)" ::: "memory"); __builtin_amdgcn_s_barrier(); asm volatile("" ::: "memory"); } while (0)
#define CFENCE() asm volatile("" ::: "memory")
#define VMWAIT(n) asm volatile("s_waitcnt vmcnt(" #n ")" ::: "memory")
__device__ __forceinline__ u32 umax4(v4u v) { u32 a = v.x > v.y ? v.x : v.y, b = v.z > v.w ? v.z : v.w; return a > b ? a : b; }
__device__ __forceinline__ v4u nosent(v4u v) {
  v.x = v.x < 0xFFFFFFFEu ? v.x : 0xFFFFFFFEu; v.y = v.y < 0xFFFFFFFEu ? v.y : 0xFFFFFFFEu;
  v.z = v.z < 0xFFFFFFFEu ? v.z : 0xFFFFFFFEu; v.w = v.w < 0xFFFFFFFEu ? v.w : 0xFFFFFFFEu; return v;
}
__device__ __forceinline__ void lds_signal(LAS unsigned char* L, int c, int lane) {
  asm volatile("s_waitcnt lgkmcnt(0)" ::: "memory");
  if (lane == 0) __hip_atomic_fetch_add((LAS u32*)(L + M_CNT) + c, 1u, __ATOMIC_RELAXED, __HIP_MEMORY_SCOPE_WORKGROUP);
}
__device__ __forceinline__ void lds_wait(LAS unsigned char* L, int c, u32 target) {
  _Pragma("unroll 1") for (u32 spins = 0; spins < (1u << 24); ++spins) {
    if (*((volatile LAS u32*)(L + M_CNT) + c) >= target) break;
    __builtin_amdgcn_s_sleep(0);
  }
  CFENCE();
}

__device__ __forceinline__ void ln_row(LAS unsigned char* L, v4u xv, v4u wrv, int r, int lane) {
  float xf[8] = {lo_f(xv.x), hi_f(xv.x), lo_f(xv.y), hi_f(xv.y), lo_f(xv.z), hi_f(xv.z), lo_f(xv.w), hi_f(xv.w)};
  float s = ((xf[0] + xf[1]) + (xf[2] + xf[3])) + ((xf[4] + xf[5]) + (xf[6] + xf[7]));
  const float mean = sum64(s) * (1.0f / 512.0f);
  float q2 = 0.f;
#pragma unroll
  for (int e = 0; e < 8; ++e) { const float d = xf[e] - mean; q2 += d * d; }
  const float var = rbf(sum64(q2) * (1.0f / 512.0f));
  const float mb = rbf(mean);
  const float den = rbf(sqrtf(rbf(var + 1e-5f)));
  float xn[8];
#pragma unroll
  for (int e = 0; e < 8; ++e) xn[e] = rbf(xf[e] - mb) / den;
  v4u xnp = {pk_bf16(xn[0], xn[1]), pk_bf16(xn[2], xn[3]), pk_bf16(xn[4], xn[5]), pk_bf16(xn[6], xn[7])};
  *(LAS v4u*)(L + L_XN + r * 1024 + lane * 16) = xnp;
  lds_signal(L, C_XN, lane);
  float rr = dot2(xnp.x, wrv.x, 0.f); rr = dot2(xnp.y, wrv.y, rr); rr = dot2(xnp.z, wrv.z, rr); rr = dot2(xnp.w, wrv.w, rr);
  rr = rbf(sum64(rr));
  if (lane == 0) *(LAS float*)(L + M_RS + r * 4) = rbf(1.0f / (1.0f + __expf(-rr)));
}

__device__ __forceinline__ u32 gsw(u32 row) { return (((row >> 1) & 1u) | (((row >> 3) & 1u) << 1)) << 1; }
__device__ __forceinline__ void tile_gemv(LAS unsigned char* L, int tile_off, int m0, int kq, int lane) {
  const int i16 = lane & 15, gq = lane >> 4, q4 = i16 >> 2, p4 = i16 & 3;
  f4 acc[4];
#pragma unroll
  for (int dt = 0; dt < 4; ++dt) acc[dt] = f4{0.f, 0.f, 0.f, 0.f};
  const u32 fl = gsw(8 * gq + q4);
#pragma unroll
  for (int t = 0; t < 2; ++t) {
    const int r0 = 64 * kq + 32 * t + 8 * gq + q4;
    const v4u xf = *(const LAS v4u*)(L + L_XN + (i16 & 3) * 1024 + (m0 + 64 * kq + 32 * t + 8 * gq) * 2);
    LAS unsigned char* b0 = L + tile_off + r0 * 128 + 8 * (p4 & 1);
#pragma unroll
    for (int dt = 0; dt < 4; ++dt) {
      const v2u a0 = trread(b0 + (((2 * dt + (p4 >> 1)) ^ fl) * 16)), a1 = trread(b0 + 512 + (((2 * dt + (p4 >> 1)) ^ fl) * 16));
      acc[dt] = mfma16(v4u{a0.x, a0.y, a1.x, a1.y}, xf, acc[dt]);
    }
  }
  if (i16 < 4) {
#pragma unroll
    for (int dt = 0; dt < 4; ++dt) *(LAS f4*)(L + M_RED + (((kq * 4 + dt) * 4 + gq) * 4 + i16) * 16) = acc[dt];
  }
}

__global__ __launch_bounds__(512, 2) void mega(const u16* __restrict__ x, const u16* __restrict__ ckv, const u16* __restrict__ wq,
                                               const u16* __restrict__ wuk, const u16* __restrict__ wuv, const u16* __restrict__ wo,
                                               const u16* __restrict__ wr, const u16* __restrict__ wg, const u16* __restrict__ wu,
                                               const u16* __restrict__ wd, u16* __restrict__ out, unsigned char* ws, u64* stamps) {
  extern __shared__ __attribute__((aligned(16))) unsigned char lds_raw[];
  LAS unsigned char* L = (LAS unsigned char*)lds_raw;
  const int tid = threadIdx.x, lane = tid & 63;
  const int wave = __builtin_amdgcn_readfirstlane(tid >> 6);
  const int g = blockIdx.x, xg = g & 7, idx = g >> 3;
  const int i16 = lane & 15, gq = lane >> 4, q4 = i16 >> 2, p4 = i16 & 3;
  const __amdgpu_buffer_rsrc_t rs = __builtin_amdgcn_make_buffer_rsrc((void*)ws, 0, (int)WS_END, 0x00020000);
  const u32 lds_base = (u32)(size_t)lds_raw;
  const v4u sv = {SENT, SENT, SENT, SENT};
#define STAMP(k)
  STAMP(0);
  if (tid < 24) *((LAS u32*)(L + M_CNT) + tid) = 0u;
  const u64 tk0 = __builtin_amdgcn_s_memrealtime();
  const v2u ep = *(const __attribute__((address_space(4))) v2u*)(ws + WS_EPOCH);

  if (idx >= 16) {
    const int wi = idx - 16, h = xg * 4 + (wi >> 2), j = wi & 3, wid = xg * 16 + wi;
    if (wave < 4) {
      const int a = wave;
      const v4u xv = *(const GAS v4u*)(x + a * 512 + lane * 8);
      const v4u wrv = *(const GAS v4u*)(wr + lane * 8);
      BAR();
      const u32 tagA = ep.x + 1u, tagB = ep.y + 1u;
      ln_row(L, xv, wrv, a, lane);
      lds_wait(L, C_XN, 4);
      lds_wait(L, C_T, 4);
      STAMP(1);
      tile_gemv(L, L_BIG, 256 * (j >> 1), a, lane);
      lds_signal(L, C_RED, lane);
      lds_wait(L, C_RED, 4);
      if (lane < 32) {
        float s0 = 0.f, s1 = 0.f;
#pragma unroll
        for (int kq = 0; kq < 4; ++kq) {
          const v2u pv = *(const LAS v2u*)(L + M_RED + ((kq * 16 + (lane >> 1)) * 4 + a) * 16 + (lane & 1) * 8);
          s0 += __uint_as_float(pv.x); s1 += __uint_as_float(pv.y);
        }
        __builtin_amdgcn_raw_buffer_store_b128(v4u{__float_as_uint(s0), tagA, __float_as_uint(s1), tagB}, rs,
                                               WS_QX + (((h * 4 + a) * 2 + (j >> 1)) * 64 + 32 * (j & 1) + lane) * 16, 0, 16);
      }
      STAMP(2);
      lds_wait(L, C_T2, 4);
      v4u wukf[2][4];
#pragma unroll
      for (int ct = 0; ct < 2; ++ct)
#pragma unroll
        for (int t = 0; t < 4; ++t)
          wukf[ct][t] = *(const LAS v4u*)(L + L_BIG + 32768 + (32 * a + 16 * ct + i16) * 256 + (((4 * t + gq) ^ swz(i16)) * 16));
      v4u qu[2];
      qu[0] = qu[1] = v4u{0u, tagA ^ 1u, 0u, 0u};
      const u32 qsrc = WS_QX + ((h * 4 + a) * 128 + lane) * 16;
      _Pragma("unroll 1") for (u32 spins = 0; spins < SPIN_CAP; ++spins) {
        CFENCE();
#pragma unroll
        for (int u = 0; u < 2; ++u)
          if (((qu[u].y ^ tagA) | (qu[u].w ^ tagB)) != 0u) qu[u] = __builtin_amdgcn_raw_buffer_load_b128(rs, qsrc + u * 1024, 0, 16);
        const u32 bad = (qu[0].y ^ tagA) | (qu[0].w ^ tagB) | (qu[1].y ^ tagA) | (qu[1].w ^ tagB);
        if (__all(bad == 0u)) break;
      }
      *(LAS u32*)(L + M_QS + a * 256 + lane * 4) = pk_bf16(__uint_as_float(qu[0].x) + __uint_as_float(qu[1].x), __uint_as_float(qu[0].z) + __uint_as_float(qu[1].z));
      lds_signal(L, C_QS, lane);
      lds_wait(L, C_QS, 4);
      STAMP(3);
      LAS unsigned char* priv = L + L_BIG + 65536 + a * 2048;
      f4 qa0 = {0.f, 0.f, 0.f, 0.f}, qa1 = qa0;
#pragma unroll
      for (int t = 0; t < 4; ++t) {
        const v4u qf = *(const LAS v4u*)(L + M_QS + (i16 & 3) * 256 + (32 * t + 8 * gq) * 2);
        qa0 = mfma16(wukf[0][t], qf, qa0);
        qa1 = mfma16(wukf[1][t], qf, qa1);
      }
      const float SC = 0.08838834764831845f * 1.4426950408889634f;
      if (i16 < 4) {
        *(LAS v2u*)(priv + 1024 + (i16 * 8 + gq) * 8) = v2u{pk_bf16(qa0[0] * SC, qa0[1] * SC), pk_bf16(qa0[2] * SC, qa0[3] * SC)};
        *(LAS v2u*)(priv + 1024 + (i16 * 8 + 4 + gq) * 8) = v2u{pk_bf16(qa1[0] * SC, qa1[1] * SC), pk_bf16(qa1[2] * SC, qa1[3] * SC)};
      }
      lds_wait(L, C_CLR, 4);
      if (lane < 32) {
        const v2u pv = *(const LAS v2u*)(priv + 1024 + lane * 8);
        __builtin_amdgcn_raw_buffer_store_b128(v4u{pv.x, tagA, pv.y, tagB}, rs, WS_QA + (((lane >> 3) * 32 + h) * 128 + 32 * j + 8 * a + (lane & 7)) * 16, 0, 16);
      }
      lds_signal(L, C_A, lane);
      STAMP(4);
    } else {
      const int lw = wave - 4, tl = tid - 256;
      BAR();
      {
        const u16* base = wq + (size_t)(256 * (j >> 1) + 64 * lw) * 4096 + h * 128 + 64 * (j & 1);
#pragma unroll
        for (int r = 0; r < 8; ++r) {
          const u32 row = 8 * r + (lane >> 3);
          glds16s(base, row * 8192u + (((u32)(lane & 7) ^ gsw(row)) * 16u), lds_base + L_BIG + (lw * 8 + r) * 1024);
        }
#pragma unroll
        for (int r = 0; r < 8; ++r) {
          const int row = (lw * 8 + r) * 4 + gq;
          glds16s(wuk + (size_t)(128 * j) * 4096 + h * 128, (u32)(row * 8192 + ((i16 ^ swz(row)) * 16)), lds_base + L_BIG + 32768 + (lw * 8 + r) * 1024);
        }
      }
      VMWAIT(8);
      lds_signal(L, C_T, lane);
      STAMP(1);
#pragma unroll
      for (int u = 0; u < 8; ++u) __builtin_amdgcn_raw_buffer_store_b128(sv, rs, WS_IN3 + (h * 4 + j) * 32768 + tl * 16, u * 4096, 16);
      __builtin_amdgcn_raw_buffer_store_b128(sv, rs, WS_INY + wid * 7168 + tl * 16, 0, 16);
      __builtin_amdgcn_raw_buffer_store_b128(sv, rs, tl < 192 ? WS_INY + wid * 7168 + (256 + tl) * 16 : WS_ST3 + h * 512 + j * 128 + (tl & 7) * 16, 0, 16);
      VMWAIT(10);
      lds_signal(L, C_T2, lane);
      VMWAIT(0);
      lds_signal(L, C_CLR, lane);
      STAMP(2);
      lds_wait(L, C_A, 4);
      while ((long long)(__builtin_amdgcn_s_memrealtime() - tk0) < WODELAY) __builtin_amdgcn_s_sleep(2);
      STAMP(3);
#pragma unroll
      for (int r = 0; r < 8; ++r) {
        const int row = (lw * 8 + r) * 4 + gq;
        glds16s(wuv + (size_t)(128 * j) * 4096 + h * 128, (u32)(row * 8192 + ((i16 ^ swz(row)) * 16)), lds_base + L_BIG + 98304 + (lw * 8 + r) * 1024);
      }
      const u16* wop = wo + (size_t)h * 128 * 512;
#pragma unroll 4
      for (int r = 0; r < 24; ++r) {
        const int row = lw * 24 + r;
        glds16s(wop + (size_t)row * 512, (u32)((lane ^ swz(row)) * 16), lds_base + L_BIG + row * 1024);
      }
      VMWAIT(24);
      lds_signal(L, C_UV, lane);
    }
    lds_wait(L, C_UV, 4);
    v4u uvf[4];
#pragma unroll
    for (int t = 0; t < 4; ++t) {
      const int r0 = 32 * t + 8 * gq + q4, r1 = r0 + 4;
      const v2u lo = trread(L + L_BIG + 98304 + r0 * 256 + (((2 * wave + (p4 >> 1)) ^ swz(r0)) * 16) + 8 * (p4 & 1));
      const v2u hi = trread(L + L_BIG + 98304 + r1 * 256 + (((2 * wave + (p4 >> 1)) ^ swz(r1)) * 16) + 8 * (p4 & 1));
      uvf[t] = v4u{lo.x, lo.y, hi.x, hi.y};
    }
    lds_signal(L, C_UVR, lane);
    STAMP(5);
    if (wave >= 4) {
      const int lw = wave - 4;
      lds_wait(L, C_UVR, 8);
      const u16* wop = wo + (size_t)h * 128 * 512;
#pragma unroll
      for (int r = 0; r < 8; ++r) {
        const int row = 96 + lw * 8 + r;
        glds16s(wop + (size_t)row * 512, (u32)((lane ^ swz(row)) * 16), lds_base + L_BIG + row * 1024);
      }
      VMWAIT(0);
      STAMP(6);
    }
    BAR();
    v4u wf[4][4];
#pragma unroll
    for (int t = 0; t < 4; ++t) {
      const int r0 = 32 * t + 8 * gq + q4, r1 = r0 + 4;
#pragma unroll
      for (int tt = 0; tt < 4; ++tt) {
        const v2u lo = trread(L + L_BIG + r0 * 1024 + (((8 * wave + 2 * tt + (p4 >> 1)) ^ swz(r0)) * 16) + 8 * (p4 & 1));
        const v2u hi = trread(L + L_BIG + r1 * 1024 + (((8 * wave + 2 * tt + (p4 >> 1)) ^ swz(r1)) * 16) + 8 * (p4 & 1));
        wf[t][tt] = v4u{lo.x, lo.y, hi.x, hi.y};
      }
    }
    STAMP(7);
    {
      const int b = wave >> 1, chh = wave & 1, chunk = lane & 7, srow = lane >> 3;
      const u32 src = WS_IN3 + (h * 4 + j) * 32768 + (b * 32 + srow) * 256 + chh * 128 + chunk * 16;
      if (wave == 0) {
        v2u lv = {SENT, SENT};
        _Pragma("unroll 1") for (u32 spins = 0; spins < SPIN_CAP; ++spins) {
          CFENCE();
          if ((lv.x > lv.y ? lv.x : lv.y) == SENT) lv = __builtin_amdgcn_raw_buffer_load_b64(rs, WS_ST3 + h * 512 + lane * 8, 0, 16);
          if (__all((lv.x > lv.y ? lv.x : lv.y) != SENT)) break;
          __builtin_amdgcn_s_sleep(2);
        }
        *(LAS v2u*)(L + M_LSE + lane * 8) = lv;
        lds_signal(L, C_LSE, lane);
      }
      lds_wait(L, C_LSE, 1);
      STAMP(10);
      v4u v[4]; u32 lb[4];
#pragma unroll
      for (int u = 0; u < 4; ++u) { v[u] = sv; lb[u] = *(const LAS u32*)(L + M_LSE + (b * 32 + 8 * u + srow) * 4); }
      _Pragma("unroll 1") for (u32 spins = 0; spins < SPIN_CAP; ++spins) {
        CFENCE();
#pragma unroll
        for (int u = 0; u < 4; ++u) if (umax4(v[u]) == SENT) v[u] = __builtin_amdgcn_raw_buffer_load_b128(rs, src + u * 2048, 0, 16);
        u32 mxv = 0u;
#pragma unroll
        for (int u = 0; u < 4; ++u) { const u32 t = umax4(v[u]); mxv = mxv > t ? mxv : t; }
        if (__all(mxv != SENT)) break;
        __builtin_amdgcn_s_sleep(2);
      }
      STAMP(8);
      float M = fmaxf(fmaxf(__uint_as_float(lb[0]), __uint_as_float(lb[1])), fmaxf(__uint_as_float(lb[2]), __uint_as_float(lb[3])));
      M = fmaxf(M, dpp<0x128>(M));
      M = maxx32(maxx16(M));
      float ex[4];
#pragma unroll
      for (int u = 0; u < 4; ++u) ex[u] = __builtin_amdgcn_exp2f(__uint_as_float(lb[u]) - M);
      float Ls = (ex[0] + ex[1]) + (ex[2] + ex[3]);
      Ls += dpp<0x128>(Ls);
      Ls = sumx32(sumx16(Ls));
      float oc[8];
#pragma unroll
      for (int e = 0; e < 8; ++e) oc[e] = 0.f;
#pragma unroll
      for (int u = 0; u < 4; ++u) {
        const float wv = ex[u];
        oc[0] += wv * lo_f(v[u].x); oc[1] += wv * hi_f(v[u].x); oc[2] += wv * lo_f(v[u].y); oc[3] += wv * hi_f(v[u].y);
        oc[4] += wv * lo_f(v[u].z); oc[5] += wv * hi_f(v[u].z); oc[6] += wv * lo_f(v[u].w); oc[7] += wv * hi_f(v[u].w);
      }
      float r4[4], r2[2];
#pragma unroll
      for (int e = 0; e < 4; ++e) r4[e] = tred32(oc[e], oc[4 + e]);
#pragma unroll
      for (int e = 0; e < 2; ++e) r2[e] = tred16(r4[e], r4[2 + e]);
      r2[0] += dpp<0x128>(r2[0]); r2[1] += dpp<0x128>(r2[1]);
      const float iL = __builtin_amdgcn_rcpf(Ls);
      if ((lane & 8) == 0) *(LAS u32*)(L + M_OCS + b * 256 + chh * 128 + chunk * 16 + gq * 4) = pk_bf16(r2[0] * iL, r2[1] * iL);
    }
    BAR();
    {
      f4 c = {0.f, 0.f, 0.f, 0.f};
#pragma unroll
      for (int t = 0; t < 4; ++t) {
        const v4u of = *(const LAS v4u*)(L + M_OCS + (i16 & 3) * 256 + (32 * t + 8 * gq) * 2);
        c = mfma16(uvf[t], of, c);
      }
      if (i16 < 4) *(LAS v2u*)(L + M_CTX + (i16 * 128 + 16 * wave + 4 * gq) * 2) = v2u{pk_bf16(c[0], c[1]), pk_bf16(c[2], c[3])};
    }
    BAR();
    {
      f4 sacc[4];
#pragma unroll
      for (int tt = 0; tt < 4; ++tt) sacc[tt] = f4{0.f, 0.f, 0.f, 0.f};
#pragma unroll
      for (int t = 0; t < 4; ++t) {
        const v4u cf = *(const LAS v4u*)(L + M_CTX + ((i16 & 3) * 128 + 32 * t + 8 * gq) * 2);
#pragma unroll
        for (int tt = 0; tt < 4; ++tt) sacc[tt] = mfma16(wf[t][tt], cf, sacc[tt]);
      }
      LAS unsigned char* priv = L + L_P + wave * 1024;
      if (i16 < 4) {
#pragma unroll
        for (int tt = 0; tt < 4; ++tt) *(LAS f4*)(priv + ((tt * 4 + i16) * 4 + gq) * 16) = sacc[tt];
      }
      const v4u o = *(const LAS v4u*)(priv + lane * 16);
      const int f = ((lane >> 2) & 3) * 32 + 4 * wave + (lane >> 4);
      __builtin_amdgcn_raw_buffer_store_b128(nosent(o), rs, WS_IN5 + (f * 128 + wid) * 64 + (lane & 3) * 16, 0, 16);
    }
    STAMP(9);
  } else {
    const int kb = xg * 16 + idx, ab = kb >> 5, as = kb & 31;
    const bool do_ffn = idx < 14;
    const int fg = idx * 8 + xg;
    const u32 tagA = ep.x + 1u, tagB = ep.y + 1u;
    if (wave < 4) {
      const v4u xv = *(const GAS v4u*)(x + wave * 512 + lane * 8);
      const v4u wrv = *(const GAS v4u*)(wr + lane * 8);
      v4u fw[8];
      const int mat = wave & 1, ks = wave >> 1;
      if (do_ffn) {
        const u16* src = (mat ? wu : wg) + (size_t)(64 * xg + 32 * ks + 8 * gq) * 1792 + 128 * idx + 8 * i16;
#pragma unroll
        for (int jj = 0; jj < 8; ++jj) fw[jj] = ldnt(src + (size_t)jj * 1792);
      }
      BAR();
      ln_row(L, xv, wrv, wave, lane);
      if (do_ffn) {
        lds_wait(L, C_XN, 4);
        const v4u af = *(const LAS v4u*)(L + L_XN + (i16 >> 2) * 1024 + (64 * xg + 32 * ks + 8 * gq) * 2);
        float pr[8];
#pragma unroll
        for (int cc = 0; cc < 8; ++cc) {
          const u32 sel = (cc & 1) ? 0x07060302u : 0x05040100u;
          v4u bfr;
          bfr.x = __builtin_amdgcn_perm(fw[1][cc >> 1], fw[0][cc >> 1], sel);
          bfr.y = __builtin_amdgcn_perm(fw[3][cc >> 1], fw[2][cc >> 1], sel);
          bfr.z = __builtin_amdgcn_perm(fw[5][cc >> 1], fw[4][cc >> 1], sel);
          bfr.w = __builtin_amdgcn_perm(fw[7][cc >> 1], fw[6][cc >> 1], sel);
          pr[cc] = mfma16(af, bfr, f4{0.f, 0.f, 0.f, 0.f})[0];
        }
        LAS f4* dst = (LAS f4*)(L + M_RED + ((wave * 4 + gq) * 128 + 8 * i16) * 4);
        dst[0] = f4{pr[0], pr[1], pr[2], pr[3]};
        dst[1] = f4{pr[4], pr[5], pr[6], pr[7]};
        lds_signal(L, C_FA, lane);
        lds_wait(L, C_FA, 4);
#pragma unroll
        for (int u = 0; u < 2; ++u) {
          const int U = ks * 128 + u * 64 + lane, msd = U >> 5, b = (U >> 3) & 3, k = U & 7;
          const v2u p0 = *(const LAS v2u*)(L + M_RED + (((0 + mat) * 4 + b) * 128 + 16 * msd + 2 * k) * 4);
          const v2u p1 = *(const LAS v2u*)(L + M_RED + (((2 + mat) * 4 + b) * 128 + 16 * msd + 2 * k) * 4);
          const v4u o = {__float_as_uint(__uint_as_float(p0.x) + __uint_as_float(p1.x)), tagA, __float_as_uint(__uint_as_float(p0.y) + __uint_as_float(p1.y)), tagB};
          __builtin_amdgcn_raw_buffer_store_b128(o, rs, WS_FX + (idx * 8 + msd) * 8192 + (((xg * 2 + mat) * 4 + b) * 8 + k) * 16, 0, 16);
        }
        STAMP(12);
      }
    } else {
      const int lw = wave - 4, tl = tid - 256;
      __builtin_amdgcn_raw_buffer_store_b128(sv, rs, WS_IN5 + kb * 8192 + tl * 16, 0, 16);
      __builtin_amdgcn_raw_buffer_store_b128(sv, rs, WS_IN5 + kb * 8192 + tl * 16, 4096, 16);
      BAR();
      while ((long long)(__builtin_amdgcn_s_memrealtime() - tk0) < KVDELAY) __builtin_amdgcn_s_sleep(1);
    }
    const u32 sz = swz(i16);
    const u16 xfin = x[ab * 512 + as * 16 + (tid & 15)];
    v4u sf[16], vf[4][4];
    {
      const u16* kvp = ckv + ((size_t)ab * 4096 + as * 128 + (wave - 4) * 8) * 512;
      const u32 ldst = lds_base + L_BIG + (wave - 4) * 8192;
      const u32 rb = (u32)(wave & 1) * 8u;
      if (wave >= 4) {
#pragma unroll
        for (int t = 0; t < 2; ++t)
#pragma unroll
          for (int r = 0; r < 8; ++r) glds16s(kvp + (size_t)(32 * t + r) * 512, (u32)((lane ^ swz(rb + r)) * 16), ldst + (32 * t + r) * 1024);
      }
#pragma unroll
      for (int t = 0; t < 4; ++t) {
        if (wave >= 4) {
          if (t < 3) VMWAIT(8); else VMWAIT(0);
          lds_signal(L, C_KQ + t, lane);
          if (t < 2) {
#pragma unroll
            for (int r = 0; r < 8; ++r) glds16s(kvp + (size_t)(32 * (t + 2) + r) * 512, (u32)((lane ^ swz(rb + r)) * 16), ldst + (32 * (t + 2) + r) * 1024);
          }
          if (t == 3) STAMP(2);
        }
        lds_wait(L, C_KQ + t, 4);
        const int k0 = 32 * t + 8 * gq + q4, k1 = k0 + 4;
#pragma unroll
        for (int tt = 0; tt < 4; ++tt) {
          const v2u lo = trread(L + L_BIG + k0 * 1024 + (((8 * wave + 2 * tt + (p4 >> 1)) ^ swz(k0)) * 16) + 8 * (p4 & 1));
          const v2u hi = trread(L + L_BIG + k1 * 1024 + (((8 * wave + 2 * tt + (p4 >> 1)) ^ swz(k1)) * 16) + 8 * (p4 & 1));
          vf[t][tt] = v4u{lo.x, lo.y, hi.x, hi.y};
        }
        if ((wave >> 1) == t) {
          LAS unsigned char* kvrow = L + L_BIG + (16 * wave + i16) * 1024;
#pragma unroll
          for (int u = 0; u < 16; ++u) sf[u] = *(const LAS v4u*)(kvrow + 256 * (u >> 2) + 16 * ((4 * (u & 3) + gq) ^ sz));
        }
        if (t == 0) lds_signal(L, C_PR, lane);
      }
    }
    STAMP(3);
    {
      const u32 src = WS_QA + (ab * 4096 + tid) * 16;
      v4u v[8];
#pragma unroll
      for (int u = 0; u < 8; ++u) v[u] = v4u{0u, tagA ^ 1u, 0u, 0u};
      _Pragma("unroll 1") for (u32 spins = 0; spins < SPIN_CAP; ++spins) {
        CFENCE();
        v[0] = __builtin_amdgcn_raw_buffer_load_b128(rs, src, 0, 16);
        if (__all(((v[0].y ^ tagA) | (v[0].w ^ tagB)) == 0u)) break;
        __builtin_amdgcn_s_sleep(2);
      }
      _Pragma("unroll 1") for (u32 spins = 0; spins < SPIN_CAP; ++spins) {
        CFENCE();
#pragma unroll
        for (int u = 1; u < 8; ++u)
          if (((v[u].y ^ tagA) | (v[u].w ^ tagB)) != 0u) v[u] = __builtin_amdgcn_raw_buffer_load_b128(rs, src, u * 8192, 16);
        u32 bad = 0u;
#pragma unroll
        for (int u = 0; u < 8; ++u) bad |= (v[u].y ^ tagA) | (v[u].w ^ tagB);
        if (__all(bad == 0u)) break;
        __builtin_amdgcn_s_sleep(1);
      }
      const int cu = tid & 127, hb = tid >> 7;
      lds_wait(L, C_PR, 8);
#pragma unroll
      for (int u = 0; u < 8; ++u) {
        const int hh = 4 * u + hb;
        *(LAS v2u*)(L + L_BIG + hh * 1024 + (((cu >> 1) ^ swz(hh)) * 16) + (cu & 1) * 8) = v2u{v[u].x, v[u].z};
      }
    }
    STAMP(4);
    BAR();
    STAMP(5);
    v4u fu = {0u, 0u, 0u, 0u}, wd0 = fu, wd1 = fu;
    if (do_ffn) {
      fu = __builtin_amdgcn_raw_buffer_load_b128(rs, WS_FX + fg * 8192 + tid * 16, 0, 16);
      wd0 = ldnt(wd + (size_t)(16 * fg + 2 * (lane >> 3)) * 512 + 64 * wave + 8 * (lane & 7));
      wd1 = ldnt(wd + (size_t)(16 * fg + 2 * (lane >> 3) + 1) * 512 + 64 * wave + 8 * (lane & 7));
    }
    __builtin_amdgcn_sched_barrier(0);
    f4 s0 = {0.f, 0.f, 0.f, 0.f}, s1 = s0;
    {
      LAS unsigned char* qrow = L + L_BIG + i16 * 1024;
#pragma unroll
      for (int t = 0; t < 16; ++t) {
        const u32 off = 256 * (t >> 2) + 16 * ((4 * (t & 3) + gq) ^ sz);
        const v4u qa = *(const LAS v4u*)(qrow + off), qb = *(const LAS v4u*)(qrow + 16384 + off);
        s0 = mfma16(sf[t], qa, s0);
        s1 = mfma16(sf[t], qb, s1);
      }
    }
    {
      float mx0 = maxx32(maxx16(fmaxf(fmaxf(s0[0], s0[1]), fmaxf(s0[2], s0[3]))));
      float mx1 = maxx32(maxx16(fmaxf(fmaxf(s1[0], s1[1]), fmaxf(s1[2], s1[3]))));
      if (lane < 32) *(LAS float*)(L + M_WMAX + (lane * 8 + wave) * 4) = lane < 16 ? mx0 : mx1;
    }
    STAMP(6);
    BAR();
    float m0, m1;
    {
      const f4 a0 = *(const LAS f4*)(L + M_WMAX + i16 * 32), a1 = *(const LAS f4*)(L + M_WMAX + i16 * 32 + 16);
      const f4 b0 = *(const LAS f4*)(L + M_WMAX + 512 + i16 * 32), b1 = *(const LAS f4*)(L + M_WMAX + 512 + i16 * 32 + 16);
      m0 = fmaxf(fmaxf(fmaxf(a0[0], a0[1]), fmaxf(a0[2], a0[3])), fmaxf(fmaxf(a1[0], a1[1]), fmaxf(a1[2], a1[3])));
      m1 = fmaxf(fmaxf(fmaxf(b0[0], b0[1]), fmaxf(b0[2], b0[3])), fmaxf(fmaxf(b1[0], b1[1]), fmaxf(b1[2], b1[3])));
      const u32 pa = pk_bf16(__builtin_amdgcn_exp2f(s0[0] - m0), __builtin_amdgcn_exp2f(s0[1] - m0));
      const u32 pb = pk_bf16(__builtin_amdgcn_exp2f(s0[2] - m0), __builtin_amdgcn_exp2f(s0[3] - m0));
      const u32 pc = pk_bf16(__builtin_amdgcn_exp2f(s1[0] - m1), __builtin_amdgcn_exp2f(s1[1] - m1));
      const u32 pd = pk_bf16(__builtin_amdgcn_exp2f(s1[2] - m1), __builtin_amdgcn_exp2f(s1[3] - m1));
      const u32 poff = i16 * 256 + (((2 * wave + (gq >> 1)) ^ sz) * 16) + (gq & 1) * 8;
      *(LAS v2u*)(L + L_P + poff) = v2u{pa, pb};
      *(LAS v2u*)(L + L_P + 4096 + poff) = v2u{pc, pd};
      const float l0 = sumx32(sumx16((lo_f(pa) + hi_f(pa)) + (lo_f(pb) + hi_f(pb))));
      const float l1 = sumx32(sumx16((lo_f(pc) + hi_f(pc)) + (lo_f(pd) + hi_f(pd))));
      if (lane < 32) *(LAS float*)(L + M_WSUM + (lane * 8 + wave) * 4) = lane < 16 ? l0 : l1;
    }
    BAR();
    STAMP(7);
    if (wave == 0 && lane < 32) {
      const f4 la = *(const LAS f4*)(L + M_WSUM + lane * 32), lb = *(const LAS f4*)(L + M_WSUM + lane * 32 + 16);
      const float l = ((la[0] + la[1]) + (la[2] + la[3])) + ((lb[0] + lb[1]) + (lb[2] + lb[3]));
      u32 lse = __float_as_uint((lane < 16 ? m0 : m1) + __builtin_amdgcn_logf(l));
      lse = lse < 0xFFFFFFFEu ? lse : 0xFFFFFFFEu;
      __builtin_amdgcn_raw_buffer_store_b32(lse, rs, WS_ST3 + ((lane * 4 + ab) * 32 + as) * 4, 0, 16);
    }
#pragma unroll
    for (int ht = 0; ht < 2; ++ht) {
      f4 oacc[4];
#pragma unroll
      for (int tt = 0; tt < 4; ++tt) oacc[tt] = f4{0.f, 0.f, 0.f, 0.f};
#pragma unroll
      for (int t = 0; t < 4; ++t) {
        const v4u pf = *(const LAS v4u*)(L + L_P + ht * 4096 + i16 * 256 + (((4 * t + gq) ^ sz) * 16));
#pragma unroll
        for (int tt = 0; tt < 4; ++tt) oacc[tt] = mfma16(vf[t][tt], pf, oacc[tt]);
      }
      const f4 la = *(const LAS f4*)(L + M_WSUM + ht * 512 + i16 * 32), lb = *(const LAS f4*)(L + M_WSUM + ht * 512 + i16 * 32 + 16);
      const float l = ((la[0] + la[1]) + (la[2] + la[3])) + ((lb[0] + lb[1]) + (lb[2] + lb[3]));
      const float il = __builtin_amdgcn_rcpf(l);
      const int head = 16 * ht + i16;
      LAS unsigned char* priv = L + L_BIG + 65536 + wave * 4608;
#pragma unroll
      for (int tt = 0; tt < 4; ++tt)
        *(LAS v2u*)(priv + head * 144 + tt * 32 + gq * 8) = v2u{pk_bf16(oacc[tt][0] * il, oacc[tt][1] * il), pk_bf16(oacc[tt][2] * il, oacc[tt][3] * il)};
#pragma unroll
      for (int e = 0; e < 2; ++e) {
        const int hd = 16 * ht + 8 * e + (lane >> 3);
        const v4u o = *(const LAS v4u*)(priv + hd * 144 + (lane & 7) * 16);
        __builtin_amdgcn_raw_buffer_store_b128(nosent(o), rs,
            WS_IN3 + (((hd * 4 + (wave >> 1)) * 4 + ab) * 32 + as) * 256 + (wave & 1) * 128 + (lane & 7) * 16, 0, 16);
      }
    }
    STAMP(8);
    if (do_ffn) {
      const int chunk = lane & 7, rg = lane >> 3;
      _Pragma("unroll 1") for (u32 spins = 0; spins < SPIN_CAP; ++spins) {
        if (__all(((fu.y ^ tagA) | (fu.w ^ tagB)) == 0u)) break;
        CFENCE();
        fu = __builtin_amdgcn_raw_buffer_load_b128(rs, WS_FX + fg * 8192 + tid * 16, 0, 16);
      }
      *(LAS v2u*)(L + L_BIG + 32768 + tid * 8) = v2u{fu.x, fu.z};
      BAR();
      if (lane < 32) {
        float g0 = 0.f, g1 = 0.f, u0 = 0.f, u1 = 0.f;
#pragma unroll
        for (int w = 0; w < 8; ++w) {
          const v2u gv = *(const LAS v2u*)(L + L_BIG + 32768 + (w * 64 + lane) * 8), uv = *(const LAS v2u*)(L + L_BIG + 32768 + (w * 64 + 32 + lane) * 8);
          g0 += __uint_as_float(gv.x); g1 += __uint_as_float(gv.y); u0 += __uint_as_float(uv.x); u1 += __uint_as_float(uv.y);
        }
        g0 = rbf(g0); g1 = rbf(g1);
        const float h0 = rbf(rbf(g0 / (1.0f + __expf(-g0))) * rbf(u0)), h1 = rbf(rbf(g1 / (1.0f + __expf(-g1))) * rbf(u1));
        *(LAS v2u*)(L + L_BIG + 40960 + wave * 256 + lane * 8) = v2u{__float_as_uint(h0), __float_as_uint(h1)};
      }
      float y[32];
#pragma unroll
      for (int b = 0; b < 4; ++b) {
        const v2u hp = *(const LAS v2u*)(L + L_BIG + 40960 + wave * 256 + (b * 16 + 2 * rg) * 4);
        const float h0 = __uint_as_float(hp.x), h1 = __uint_as_float(hp.y);
#pragma unroll
        for (int e = 0; e < 4; ++e) {
          y[b * 8 + 2 * e] = h0 * lo_f(wd0[e]) + h1 * lo_f(wd1[e]);
          y[b * 8 + 2 * e + 1] = h0 * hi_f(wd0[e]) + h1 * hi_f(wd1[e]);
        }
      }
      float n16[16], n8[8];
#pragma unroll
      for (int k = 0; k < 16; ++k) n16[k] = tred32(y[k], y[16 + k]);
#pragma unroll
      for (int k = 0; k < 8; ++k) { n8[k] = tred16(n16[k], n16[8 + k]); n8[k] += dpp<0x128>(n8[k]); }
      LAS unsigned char* priv = L + L_BIG + 65536 + wave * 4608;
      const f4 oh = (lane & 8) ? f4{n8[4], n8[5], n8[6], n8[7]} : f4{n8[0], n8[1], n8[2], n8[3]};
      *(LAS f4*)(priv + (gq * 16 + chunk * 2 + ((lane >> 3) & 1)) * 16) = oh;
      const v4u o = *(const LAS v4u*)(priv + lane * 16);
      const int f = gq * 32 + 4 * wave + (i16 >> 2);
      __builtin_amdgcn_raw_buffer_store_b128(nosent(o), rs, WS_INY + (f * 112 + fg) * 64 + (i16 & 3) * 16, 0, 16);
    }
    STAMP(9);
    {
      const u32 s5 = WS_IN5 + kb * 8192 + tid * 16;
      const u32 sy = WS_INY + kb * 7168 + (tid < 448 ? tid : 0) * 16;
      v4u a0 = sv, y0 = sv;
      _Pragma("unroll 1") for (u32 spins = 0; spins < SPIN_CAP; ++spins) {
        CFENCE();
        if (umax4(a0) == SENT) a0 = __builtin_amdgcn_raw_buffer_load_b128(rs, s5, 0, 16);
        if (umax4(y0) == SENT) y0 = __builtin_amdgcn_raw_buffer_load_b128(rs, sy, 0, 16);
        const u32 ma = umax4(a0), my = umax4(y0);
        if (__all((ma > my ? ma : my) != SENT)) break;
        __builtin_amdgcn_s_sleep(1);
      }
      STAMP(10);
      float v[8];
#pragma unroll
      for (int e = 0; e < 4; ++e) { v[e] = __uint_as_float(a0[e]); v[4 + e] = tid < 448 ? __uint_as_float(y0[e]) : 0.f; }
      float n4[4], n2[2];
#pragma unroll
      for (int e = 0; e < 4; ++e) n4[e] = tred32(v[e], v[4 + e]);
#pragma unroll
      for (int e = 0; e < 2; ++e) { n2[e] = tred16(n4[e], n4[2 + e]); n2[e] += dpp<0x124>(n2[e]); n2[e] += dpp<0x128>(n2[e]); }
      if (i16 < 4) *(LAS v2u*)(L + M_FIN + (wave * 32 + (gq >> 1) * 16 + i16 * 4 + 2 * (gq & 1)) * 4) = v2u{__float_as_uint(n2[0]), __float_as_uint(n2[1])};
    }
    BAR();
    if (tid < 16) {
      const int m = as * 16 + tid;
      float s = 0.f, y = 0.f;
#pragma unroll
      for (int w = 0; w < 8; ++w) { s += *(const LAS float*)(L + M_FIN + (w * 32 + tid) * 4); y += *(const LAS float*)(L + M_FIN + (w * 32 + 16 + tid) * 4); }
      const float rsv = *(const LAS float*)(L + M_RS + ab * 4);
      const float xnv = bf2f(*(const LAS u16*)(L + L_XN + (ab * 512 + m) * 2));
      s = rbf(s);
      y = rbf(rbf(y) * rsv);
      const float z = rbf(xnv + y);
      const float gt = rbf(z / (1.0f + __expf(-z)));
      const float o = bf2f(xfin) + rbf(s * gt);
      out[ab * 512 + m] = __builtin_bit_cast(u16, (__bf16)o);
    }
    if (g == 0 && tid == 0) __builtin_amdgcn_raw_buffer_store_b64(v2u{tagA, tagB}, rs, WS_EPOCH, 0, 16);
  }
}

extern "C" void kernel_launch(void* const* d_in, const int* in_sizes, int n_in,
                              void* d_out, int out_size, void* d_ws, size_t ws_size,
                              hipStream_t stream) {
  (void)in_sizes; (void)n_in; (void)out_size;
  (void)hipFuncSetAttribute((const void*)mega, hipFuncAttributeMaxDynamicSharedMemorySize, LDS_BYTES);
  u64* st = (u64*)((char*)d_ws + ws_size - (1 << 20));
  mega<<<NWG, 512, LDS_BYTES, stream>>>((const u16*)d_in[0], (const u16*)d_in[1], (const u16*)d_in[2], (const u16*)d_in[3],
                                        (const u16*)d_in[4], (const u16*)d_in[5], (const u16*)d_in[7], (const u16*)d_in[8],
                                        (const u16*)d_in[9], (const u16*)d_in[10], (u16*)d_out, (unsigned char*)d_ws, st);
}
```

```cpp
#include <hip/hip_runtime.h>
#include <stdint.h>

typedef unsigned short u16;
typedef unsigned int u32;
typedef unsigned long long u64;
typedef u32 v4u __attribute__((ext_vector_type(4)));
typedef u32 v2u __attribute__((ext_vector_type(2)));
typedef float f4 __attribute__((ext_vector_type(4)));
typedef short bf16x8 __attribute__((ext_vector_type(8)));
typedef short s16x4 __attribute__((ext_vector_type(4)));
typedef __bf16 bf16x2 __attribute__((ext_vector_type(2)));
#define LAS __attribute__((address_space(3)))
#define GAS __attribute__((address_space(1)))

#define STAMPS 0

#define WODELAY 400
#define CLRPOS 3
#define KDEPTH 3
#define CPF 1
#define CPF_KB 22
#define KVDELAY 100
constexpr int NWG = 256;
constexpr u32 WS_EPOCH = 0;
constexpr u32 WS_CF = 2048;
constexpr u32 WS_QX = 4096;
constexpr u32 WS_FX = WS_QX + 32 * 8192;
constexpr u32 WS_QA = WS_FX + 112 * 8192;
constexpr u32 WS_IN3 = WS_QA + 4 * 32 * 128 * 16;
constexpr u32 WS_ST3 = WS_IN3 + 32 * 4 * 32768;
constexpr u32 WS_IN5 = WS_ST3 + 32 * 128 * 4;
constexpr u32 WS_INY = WS_IN5 + 128 * 8192;
constexpr u32 WS_END = WS_INY + 128 * 7168;
constexpr int L_BIG = 0;
constexpr int L_XN = 131072;
constexpr int L_P = L_XN + 4096;
constexpr int L_M = L_P + 8192;
constexpr int M_RED = L_P;
constexpr int M_QS = L_M;
constexpr int M_LSE = L_M + 1024;
constexpr int M_WMAX = L_M + 2048;
constexpr int M_WSUM = L_M + 3072;
constexpr int M_OCS = L_M + 4096;
constexpr int M_CTX = L_M + 5120;
constexpr int M_H = L_M + 6144;
constexpr int M_FIN = L_M + 6400;
constexpr int M_RS = L_M + 7424;
constexpr int M_CNT = L_M + 7488;
constexpr int L_DUMMY = L_M + 7680;
constexpr int LDS_BYTES = L_DUMMY + 1024;
enum { C_XN = 0, C_T = 1, C_RED = 2, C_CLR = 3, C_KV = 4, C_A = 5, C_UV = 6, C_UVR = 7, C_F = 8, C_FR = 9, C_H = 10, C_T2 = 11, C_QS = 12, C_LSE = 13, C_FA = 14, C_PR = 15, C_KQ = 16   };
constexpr u32 SENT = 0xFFFFFFFFu;
constexpr u32 SPIN_CAP = 1u << 16;

__device__ __forceinline__ float bf2f(u32 v) { return __uint_as_float(v << 16); }
__device__ __forceinline__ float lo_f(u32 v) { return __uint_as_float(v << 16); }
__device__ __forceinline__ float hi_f(u32 v) { return __uint_as_float(v & 0xffff0000u); }
__device__ __forceinline__ u32 pk_bf16(float a, float b) {
  bf16x2 r; r[0] = (__bf16)a; r[1] = (__bf16)b; return __builtin_bit_cast(u32, r);
}
__device__ __forceinline__ float rbf(float a) { return (float)((__bf16)a); }
__device__ __forceinline__ f4 mfma16(v4u a, v4u b, f4 c) {
  return __builtin_amdgcn_mfma_f32_16x16x32_bf16(__builtin_bit_cast(bf16x8, a), __builtin_bit_cast(bf16x8, b), c, 0, 0, 0);
}
__device__ __forceinline__ float dot2(u32 a, u32 b, float c) {
  return __builtin_amdgcn_fdot2_f32_bf16(__builtin_bit_cast(bf16x2, a), __builtin_bit_cast(bf16x2, b), c, false);
}
__device__ __forceinline__ u32 swz(u32 row) { return ((((row >> 3) ^ (row >> 2)) & 1u) << 3) | ((row & 3u) << 1) | ((row >> 3) & 1u); }
__device__ __forceinline__ v2u trread(LAS unsigned char* p) {
  return __builtin_bit_cast(v2u, __builtin_amdgcn_ds_read_tr16_b64_v4i16((LAS s16x4*)p));
}
__device__ __forceinline__ void glds16s(const void* sbase, u32 voff, u32 lds_dst) {
  u32 keep;
  asm volatile("s_mov_b32 %0, m0\n\ts_mov_b32 m0, %3\n\ts_nop 0\n\tglobal_load_lds_dwordx4 %1, %2 nt\n\ts_mov_b32 m0, %0"
               : "=&s"(keep) : "v"(voff), "s"(sbase), "s"(lds_dst) : "memory");
}
__device__ __forceinline__ void glds16v(const void* gsrc, u32 lds_dst) {
  u32 keep;
  asm volatile("s_mov_b32 %0, m0\n\ts_mov_b32 m0, %2\n\ts_nop 0\n\tglobal_load_lds_dwordx4 %1, off nt\n\ts_mov_b32 m0, %0"
               : "=&s"(keep) : "v"(gsrc), "s"(lds_dst) : "memory");
}
__device__ __forceinline__ v4u ldnt(const void* p) { return __builtin_nontemporal_load((const GAS v4u*)p); }
template <int CTRL> __device__ __forceinline__ float dpp(float v) {
  return __int_as_float(__builtin_amdgcn_update_dpp(0, __float_as_int(v), CTRL, 0xf, 0xf, true));
}
__device__ __forceinline__ float sum16(float v) { v += dpp<0xB1>(v); v += dpp<0x4E>(v); v += dpp<0x141>(v); v += dpp<0x140>(v); return v; }
__device__ __forceinline__ float sumx16(float v) { auto r = __builtin_amdgcn_permlane16_swap(__float_as_uint(v), __float_as_uint(v), false, false); return __uint_as_float(r[0]) + __uint_as_float(r[1]); }
__device__ __forceinline__ float sumx32(float v) { auto r = __builtin_amdgcn_permlane32_swap(__float_as_uint(v), __float_as_uint(v), false, false); return __uint_as_float(r[0]) + __uint_as_float(r[1]); }
__device__ __forceinline__ float maxx16(float v) { auto r = __builtin_amdgcn_permlane16_swap(__float_as_uint(v), __float_as_uint(v), false, false); return fmaxf(__uint_as_float(r[0]), __uint_as_float(r[1])); }
__device__ __forceinline__ float maxx32(float v) { auto r = __builtin_amdgcn_permlane32_swap(__float_as_uint(v), __float_as_uint(v), false, false); return fmaxf(__uint_as_float(r[0]), __uint_as_float(r[1])); }
__device__ __forceinline__ float sum64(float v) { return sumx32(sumx16(sum16(v))); }
__device__ __forceinline__ float tred32(float a, float b) { auto r = __builtin_amdgcn_permlane32_swap(__float_as_uint(a), __float_as_uint(b), false, false); return __uint_as_float(r[0]) + __uint_as_float(r[1]); }
__device__ __forceinline__ float tred16(float a, float b) { auto r = __builtin_amdgcn_permlane16_swap(__float_as_uint(a), __float_as_uint(b), false, false); return __uint_as_float(r[0]) + __uint_as_float(r[1]); }

#define BAR() do { asm volatile("s_waitcnt lgkmcnt(0)" ::: "memory"); __builtin_amdgcn_s_barrier(); asm volatile("" ::: "memory"); } while (0)
#define CFENCE() asm volatile("" ::: "memory")
#define VMWAIT(n) asm volatile("s_waitcnt vmcnt(" #n ")" ::: "memory")
__device__ __forceinline__ u32 umax4(v4u v) { u32 a = v.x > v.y ? v.x : v.y, b = v.z > v.w ? v.z : v.w; return a > b ? a : b; }
__device__ __forceinline__ v4u nosent(v4u v) {
  v.x = v.x < 0xFFFFFFFEu ? v.x : 0xFFFFFFFEu; v.y = v.y < 0xFFFFFFFEu ? v.y : 0xFFFFFFFEu;
  v.z = v.z < 0xFFFFFFFEu ? v.z : 0xFFFFFFFEu; v.w = v.w < 0xFFFFFFFEu ? v.w : 0xFFFFFFFEu; return v;
}
__device__ __forceinline__ void lds_signal(LAS unsigned char* L, int c, int lane) {
  asm volatile("s_waitcnt lgkmcnt(0)" ::: "memory");
  if (lane == 0) __hip_atomic_fetch_add((LAS u32*)(L + M_CNT) + c, 1u, __ATOMIC_RELAXED, __HIP_MEMORY_SCOPE_WORKGROUP);
}
__device__ __forceinline__ void lds_wait(LAS unsigned char* L, int c, u32 target) {
  _Pragma("unroll 1") for (u32 spins = 0; spins < (1u << 24); ++spins) {
    if (*((volatile LAS u32*)(L + M_CNT) + c) >= target) break;
    __builtin_amdgcn_s_sleep(0);
  }
  CFENCE();
}

__device__ __forceinline__ void ln_row(LAS unsigned char* L, v4u xv, v4u wrv, int r, int lane) {
  float xf[8] = {lo_f(xv.x), hi_f(xv.x), lo_f(xv.y), hi_f(xv.y), lo_f(xv.z), hi_f(xv.z), lo_f(xv.w), hi_f(xv.w)};
  float s = ((xf[0] + xf[1]) + (xf[2] + xf[3])) + ((xf[4] + xf[5]) + (xf[6] + xf[7]));
  const float mean = sum64(s) * (1.0f / 512.0f);
  float q2 = 0.f;
#pragma unroll
  for (int e = 0; e < 8; ++e) { const float d = xf[e] - mean; q2 += d * d; }
  const float var = rbf(sum64(q2) * (1.0f / 512.0f));
  const float mb = rbf(mean);
  const float den = rbf(sqrtf(rbf(var + 1e-5f)));
  float xn[8];
#pragma unroll
  for (int e = 0; e < 8; ++e) xn[e] = rbf(xf[e] - mb) / den;
  v4u xnp = {pk_bf16(xn[0], xn[1]), pk_bf16(xn[2], xn[3]), pk_bf16(xn[4], xn[5]), pk_bf16(xn[6], xn[7])};
  *(LAS v4u*)(L + L_XN + r * 1024 + lane * 16) = xnp;
  lds_signal(L, C_XN, lane);
  float rr = dot2(xnp.x, wrv.x, 0.f); rr = dot2(xnp.y, wrv.y, rr); rr = dot2(xnp.z, wrv.z, rr); rr = dot2(xnp.w, wrv.w, rr);
  rr = rbf(sum64(rr));
  if (lane == 0) *(LAS float*)(L + M_RS + r * 4) = rbf(1.0f / (1.0f + __expf(-rr)));
}

__device__ __forceinline__ u32 gsw(u32 row) { return (((row >> 1) & 1u) | (((row >> 3) & 1u) << 1)) << 1; }
__device__ __forceinline__ void tile_gemv(LAS unsigned char* L, int tile_off, int m0, int kq, int lane) {
  const int i16 = lane & 15, gq = lane >> 4, q4 = i16 >> 2, p4 = i16 & 3;
  f4 acc[4];
#pragma unroll
  for (int dt = 0; dt < 4; ++dt) acc[dt] = f4{0.f, 0.f, 0.f, 0.f};
  const u32 fl = gsw(8 * gq + q4);
#pragma unroll
  for (int t = 0; t < 2; ++t) {
    const int r0 = 64 * kq + 32 * t + 8 * gq + q4;
    const v4u xf = *(const LAS v4u*)(L + L_XN + (i16 & 3) * 1024 + (m0 + 64 * kq + 32 * t + 8 * gq) * 2);
    LAS unsigned char* b0 = L + tile_off + r0 * 128 + 8 * (p4 & 1);
#pragma unroll
    for (int dt = 0; dt < 4; ++dt) {
      const v2u a0 = trread(b0 + (((2 * dt + (p4 >> 1)) ^ fl) * 16)), a1 = trread(b0 + 512 + (((2 * dt + (p4 >> 1)) ^ fl) * 16));
      acc[dt] = mfma16(v4u{a0.x, a0.y, a1.x, a1.y}, xf, acc[dt]);
    }
  }
  if (i16 < 4) {
#pragma unroll
    for (int dt = 0; dt < 4; ++dt) *(LAS f4*)(L + M_RED + (((kq * 4 + dt) * 4 + gq) * 4 + i16) * 16) = acc[dt];
  }
}

__global__ __launch_bounds__(512, 2) void mega(const u16* __restrict__ x, const u16* __restrict__ ckv, const u16* __restrict__ wq,
                                               const u16* __restrict__ wuk, const u16* __restrict__ wuv, const u16* __restrict__ wo,
                                               const u16* __restrict__ wr, const u16* __restrict__ wg, const u16* __restrict__ wu,
                                               const u16* __restrict__ wd, u16* __restrict__ out, unsigned char* ws, u64* stamps) {
  extern __shared__ __attribute__((aligned(16))) unsigned char lds_raw[];
  LAS unsigned char* L = (LAS unsigned char*)lds_raw;
  const int tid = threadIdx.x, lane = tid & 63;
  const int wave = __builtin_amdgcn_readfirstlane(tid >> 6);
  const int g = blockIdx.x, xg = g & 7, idx = g >> 3;
  const int i16 = lane & 15, gq = lane >> 4, q4 = i16 >> 2, p4 = i16 & 3;
  const __amdgpu_buffer_rsrc_t rs = __builtin_amdgcn_make_buffer_rsrc((void*)ws, 0, (int)WS_END, 0x00020000);
  const u32 lds_base = (u32)(size_t)lds_raw;
  const v4u sv = {SENT, SENT, SENT, SENT};
#define STAMP(k)
  asm volatile("" :: "s"(x), "s"(ckv), "s"(wq), "s"(wuk), "s"(wuv), "s"(wo), "s"(wr), "s"(wg), "s"(wu), "s"(wd), "s"(out), "s"(ws));
  {
    u64 pc;
    asm volatile("s_getpc_b64 %0" : "=s"(pc));
    const int slice = idx + 32 * (wave >> 2);
    if ((wave & 3) == 3 && slice < CPF_KB) {
      u32 keep;
      asm volatile("s_mov_b32 %0, m0\n\ts_mov_b32 m0, %3\n\ts_nop 0\n\tglobal_load_lds_dwordx4 %1, %2\n\ts_mov_b32 m0, %0"
                   : "=&s"(keep) : "v"((u32)(slice * 1024 + lane * 16)), "s"(pc & ~(u64)63), "s"(lds_base + L_DUMMY) : "memory");
    }
  }
  STAMP(0);
  if (tid < 24) *((LAS u32*)(L + M_CNT) + tid) = 0u;
  const u64 tk0 = __builtin_amdgcn_s_memrealtime();
  const v2u ep = *(const __attribute__((address_space(4))) v2u*)(ws + WS_EPOCH);

  if (idx >= 16) {
    const int wi = idx - 16, h = xg * 4 + (wi >> 2), j = wi & 3, wid = xg * 16 + wi;
    if (wave < 4) {
      const int a = wave;
      const v4u xv = *(const GAS v4u*)(x + a * 512 + lane * 8);
      const v4u wrv = *(const GAS v4u*)(wr + lane * 8);
      BAR();
      const u32 tagA = ep.x + 1u, tagB = ep.y + 1u;
      ln_row(L, xv, wrv, a, lane);
      lds_wait(L, C_XN, 4);
      lds_wait(L, C_T, 4);
      STAMP(1);
      tile_gemv(L, L_BIG, 256 * (j >> 1), a, lane);
      lds_signal(L, C_RED, lane);
      lds_wait(L, C_RED, 4);
      if (lane < 32) {
        float s0 = 0.f, s1 = 0.f;
#pragma unroll
        for (int kq = 0; kq < 4; ++kq) {
          const v2u pv = *(const LAS v2u*)(L + M_RED + ((kq * 16 + (lane >> 1)) * 4 + a) * 16 + (lane & 1) * 8);
          s0 += __uint_as_float(pv.x); s1 += __uint_as_float(pv.y);
        }
        __builtin_amdgcn_raw_buffer_store_b128(v4u{__float_as_uint(s0), tagA, __float_as_uint(s1), tagB}, rs,
                                               WS_QX + (((h * 4 + a) * 2 + (j >> 1)) * 64 + 32 * (j & 1) + lane) * 16, 0, 16);
      }
      STAMP(2);
      lds_wait(L, C_T2, 4);
      v4u wukf[2][4];
#pragma unroll
      for (int ct = 0; ct < 2; ++ct)
#pragma unroll
        for (int t = 0; t < 4; ++t)
          wukf[ct][t] = *(const LAS v4u*)(L + L_BIG + 32768 + (32 * a + 16 * ct + i16) * 256 + (((4 * t + gq) ^ swz(i16)) * 16));
      v4u qu[2];
      qu[0] = qu[1] = v4u{0u, tagA ^ 1u, 0u, 0u};
      const u32 qsrc = WS_QX + ((h * 4 + a) * 128 + lane) * 16;
      _Pragma("unroll 1") for (u32 spins = 0; spins < SPIN_CAP; ++spins) {
        CFENCE();
#pragma unroll
        for (int u = 0; u < 2; ++u)
          if (((qu[u].y ^ tagA) | (qu[u].w ^ tagB)) != 0u) qu[u] = __builtin_amdgcn_raw_buffer_load_b128(rs, qsrc + u * 1024, 0, 16);
        const u32 bad = (qu[0].y ^ tagA) | (qu[0].w ^ tagB) | (qu[1].y ^ tagA) | (qu[1].w ^ tagB);
        if (__all(bad == 0u)) break;
      }
      *(LAS u32*)(L + M_QS + a * 256 + lane * 4) = pk_bf16(__uint_as_float(qu[0].x) + __uint_as_float(qu[1].x), __uint_as_float(qu[0].z) + __uint_as_float(qu[1].z));
      lds_signal(L, C_QS, lane);
      lds_wait(L, C_QS, 4);
      STAMP(3);
      LAS unsigned char* priv = L + L_BIG + 65536 + a * 2048;
      f4 qa0 = {0.f, 0.f, 0.f, 0.f}, qa1 = qa0;
#pragma unroll
      for (int t = 0; t < 4; ++t) {
        const v4u qf = *(const LAS v4u*)(L + M_QS + (i16 & 3) * 256 + (32 * t + 8 * gq) * 2);
        qa0 = mfma16(wukf[0][t], qf, qa0);
        qa1 = mfma16(wukf[1][t], qf, qa1);
      }
      const float SC = 0.08838834764831845f * 1.4426950408889634f;
      if (i16 < 4) {
        *(LAS v2u*)(priv + 1024 + (i16 * 8 + gq) * 8) = v2u{pk_bf16(qa0[0] * SC, qa0[1] * SC), pk_bf16(qa0[2] * SC, qa0[3] * SC)};
        *(LAS v2u*)(priv + 1024 + (i16 * 8 + 4 + gq) * 8) = v2u{pk_bf16(qa1[0] * SC, qa1[1] * SC), pk_bf16(qa1[2] * SC, qa1[3] * SC)};
      }
      lds_wait(L, C_CLR, 4);
      if (lane < 32) {
        const v2u pv = *(const LAS v2u*)(priv + 1024 + lane * 8);
        __builtin_amdgcn_raw_buffer_store_b128(v4u{pv.x, tagA, pv.y, tagB}, rs, WS_QA + (((lane >> 3) * 32 + h) * 128 + 32 * j + 8 * a + (lane & 7)) * 16, 0, 16);
      }
      lds_signal(L, C_A, lane);
      STAMP(4);
      if (wi < 14) {
        while ((long long)(__builtin_amdgcn_s_memrealtime() - tk0) < WODELAY) __builtin_amdgcn_s_sleep(2);
        v4u fw[8];
        const int mat = wave & 1, ks = wave >> 1;
        const u16* src = (mat ? wu : wg) + (size_t)(64 * xg + 32 * ks + 8 * gq) * 1792 + 128 * wi + 8 * i16;
#pragma unroll
        for (int jj = 0; jj < 8; ++jj) fw[jj] = ldnt(src + (size_t)jj * 1792);
        const v4u af = *(const LAS v4u*)(L + L_XN + (i16 >> 2) * 1024 + (64 * xg + 32 * ks + 8 * gq) * 2);
        float pr[8];
#pragma unroll
        for (int cc = 0; cc < 8; ++cc) {
          const u32 sel = (cc & 1) ? 0x07060302u : 0x05040100u;
          v4u bfr;
          bfr.x = __builtin_amdgcn_perm(fw[1][cc >> 1], fw[0][cc >> 1], sel);
          bfr.y = __builtin_amdgcn_perm(fw[3][cc >> 1], fw[2][cc >> 1], sel);
          bfr.z = __builtin_amdgcn_perm(fw[5][cc >> 1], fw[4][cc >> 1], sel);
          bfr.w = __builtin_amdgcn_perm(fw[7][cc >> 1], fw[6][cc >> 1], sel);
          pr[cc] = mfma16(af, bfr, f4{0.f, 0.f, 0.f, 0.f})[0];
        }
        LAS f4* dst = (LAS f4*)(L + M_RED + ((wave * 4 + gq) * 128 + 8 * i16) * 4);
        dst[0] = f4{pr[0], pr[1], pr[2], pr[3]};
        dst[1] = f4{pr[4], pr[5], pr[6], pr[7]};
        lds_signal(L, C_FA, lane);
        lds_wait(L, C_FA, 4);
#pragma unroll
        for (int u = 0; u < 2; ++u) {
          const int U = ks * 128 + u * 64 + lane, msd = U >> 5, b = (U >> 3) & 3, k = U & 7;
          const v2u p0 = *(const LAS v2u*)(L + M_RED + (((0 + mat) * 4 + b) * 128 + 16 * msd + 2 * k) * 4);
          const v2u p1 = *(const LAS v2u*)(L + M_RED + (((2 + mat) * 4 + b) * 128 + 16 * msd + 2 * k) * 4);
          const v4u o = {__float_as_uint(__uint_as_float(p0.x) + __uint_as_float(p1.x)), tagA, __float_as_uint(__uint_as_float(p0.y) + __uint_as_float(p1.y)), tagB};
          __builtin_amdgcn_raw_buffer_store_b128(o, rs, WS_FX + (wi * 8 + msd) * 8192 + (((xg * 2 + mat) * 4 + b) * 8 + k) * 16, 0, 16);
        }
        STAMP(12);
      }
    } else {
      const int lw = wave - 4, tl = tid - 256;
      BAR();
      {
        const u16* base = wq + (size_t)(256 * (j >> 1) + 64 * lw) * 4096 + h * 128 + 64 * (j & 1);
#pragma unroll
        for (int r = 0; r < 8; ++r) {
          const u32 row = 8 * r + (lane >> 3);
          glds16s(base, row * 8192u + (((u32)(lane & 7) ^ gsw(row)) * 16u), lds_base + L_BIG + (lw * 8 + r) * 1024);
        }
#pragma unroll
        for (int r = 0; r < 8; ++r) {
          const int row = (lw * 8 + r) * 4 + gq;
          glds16s(wuk + (size_t)(128 * j) * 4096 + h * 128, (u32)(row * 8192 + ((i16 ^ swz(row)) * 16)), lds_base + L_BIG + 32768 + (lw * 8 + r) * 1024);
        }
      }
      VMWAIT(8);
      lds_signal(L, C_T, lane);
      STAMP(1);
#pragma unroll
      for (int u = 0; u < 8; ++u) __builtin_amdgcn_raw_buffer_store_b128(sv, rs, WS_IN3 + wid * 32768 + tl * 16, u * 4096, 16);
      __builtin_amdgcn_raw_buffer_store_b128(sv, rs, WS_INY + wid * 7168 + tl * 16, 0, 16);
      __builtin_amdgcn_raw_buffer_store_b128(sv, rs, tl < 192 ? WS_INY + wid * 7168 + (256 + tl) * 16 : WS_ST3 + h * 512 + (tl & 31) * 16, 0, 16);
      VMWAIT(10);
      lds_signal(L, C_T2, lane);
      VMWAIT(0);
      lds_signal(L, C_CLR, lane);
      STAMP(2);
      lds_wait(L, C_A, 4);
      while ((long long)(__builtin_amdgcn_s_memrealtime() - tk0) < WODELAY) __builtin_amdgcn_s_sleep(2);
      STAMP(3);
#pragma unroll
      for (int r = 0; r < 8; ++r) {
        const int row = (lw * 8 + r) * 4 + gq;
        glds16s(wuv + (size_t)(128 * j) * 4096 + h * 128, (u32)(row * 8192 + ((i16 ^ swz(row)) * 16)), lds_base + L_BIG + 98304 + (lw * 8 + r) * 1024);
      }
      const u16* wop = wo + (size_t)h * 128 * 512;
#pragma unroll 4
      for (int r = 0; r < 24; ++r) {
        const int row = lw * 24 + r;
        glds16s(wop + (size_t)row * 512, (u32)((lane ^ swz(row)) * 16), lds_base + L_BIG + row * 1024);
      }
      VMWAIT(24);
      lds_signal(L, C_UV, lane);
    }
    lds_wait(L, C_UV, 4);
    v4u uvf[4];
#pragma unroll
    for (int t = 0; t < 4; ++t) {
      const int r0 = 32 * t + 8 * gq + q4, r1 = r0 + 4;
      const v2u lo = trread(L + L_BIG + 98304 + r0 * 256 + (((2 * wave + (p4 >> 1)) ^ swz(r0)) * 16) + 8 * (p4 & 1));
      const v2u hi = trread(L + L_BIG + 98304 + r1 * 256 + (((2 * wave + (p4 >> 1)) ^ swz(r1)) * 16) + 8 * (p4 & 1));
      uvf[t] = v4u{lo.x, lo.y, hi.x, hi.y};
    }
    lds_signal(L, C_UVR, lane);
    STAMP(5);
    if (wave >= 4) {
      const int lw = wave - 4;
      lds_wait(L, C_UVR, 8);
      const u16* wop = wo + (size_t)h * 128 * 512;
#pragma unroll
      for (int r = 0; r < 8; ++r) {
        const int row = 96 + lw * 8 + r;
        glds16s(wop + (size_t)row * 512, (u32)((lane ^ swz(row)) * 16), lds_base + L_BIG + row * 1024);
      }
      VMWAIT(0);
      STAMP(6);
    }
    BAR();
    v4u wf[4][4];
#pragma unroll
    for (int t = 0; t < 4; ++t) {
      const int r0 = 32 * t + 8 * gq + q4, r1 = r0 + 4;
#pragma unroll
      for (int tt = 0; tt < 4; ++tt) {
        const v2u lo = trread(L + L_BIG + r0 * 1024 + (((8 * wave + 2 * tt + (p4 >> 1)) ^ swz(r0)) * 16) + 8 * (p4 & 1));
        const v2u hi = trread(L + L_BIG + r1 * 1024 + (((8 * wave + 2 * tt + (p4 >> 1)) ^ swz(r1)) * 16) + 8 * (p4 & 1));
        wf[t][tt] = v4u{lo.x, lo.y, hi.x, hi.y};
      }
    }
    STAMP(7);
    {
      const int b = wave >> 1, chh = wave & 1, chunk = lane & 7, srow = lane >> 3;
      const u32 src = WS_IN3 + (h * 4 + j) * 32768 + (b * 32 + srow) * 256 + chh * 128 + chunk * 16;
      if (wave == 0) {
        v2u lv = {SENT, SENT};
        _Pragma("unroll 1") for (u32 spins = 0; spins < SPIN_CAP; ++spins) {
          CFENCE();
          if ((lv.x > lv.y ? lv.x : lv.y) == SENT) lv = __builtin_amdgcn_raw_buffer_load_b64(rs, WS_ST3 + h * 512 + lane * 8, 0, 16);
          if (__all((lv.x > lv.y ? lv.x : lv.y) != SENT)) break;
          __builtin_amdgcn_s_sleep(2);
        }
        *(LAS v2u*)(L + M_LSE + lane * 8) = lv;
        lds_signal(L, C_LSE, lane);
      }
      lds_wait(L, C_LSE, 1);
      STAMP(10);
      v4u v[4]; u32 lb[4];
#pragma unroll
      for (int u = 0; u < 4; ++u) { v[u] = sv; lb[u] = *(const LAS u32*)(L + M_LSE + (b * 32 + 8 * u + srow) * 4); }
      _Pragma("unroll 1") for (u32 spins = 0; spins < SPIN_CAP; ++spins) {
        CFENCE();
#pragma unroll
        for (int u = 0; u < 4; ++u) if (umax4(v[u]) == SENT) v[u] = __builtin_amdgcn_raw_buffer_load_b128(rs, src + u * 2048, 0, 16);
        u32 mxv = 0u;
#pragma unroll
        for (int u = 0; u < 4; ++u) { const u32 t = umax4(v[u]); mxv = mxv > t ? mxv : t; }
        if (__all(mxv != SENT)) break;
        __builtin_amdgcn_s_sleep(2);
      }
      STAMP(8);
      float M = fmaxf(fmaxf(__uint_as_float(lb[0]), __uint_as_float(lb[1])), fmaxf(__uint_as_float(lb[2]), __uint_as_float(lb[3])));
      M = fmaxf(M, dpp<0x128>(M));
      M = maxx32(maxx16(M));
      float ex[4];
#pragma unroll
      for (int u = 0; u < 4; ++u) ex[u] = __builtin_amdgcn_exp2f(__uint_as_float(lb[u]) - M);
      float Ls = (ex[0] + ex[1]) + (ex[2] + ex[3]);
      Ls += dpp<0x128>(Ls);
      Ls = sumx32(sumx16(Ls));
      float oc[8];
#pragma unroll
      for (int e = 0; e < 8; ++e) oc[e] = 0.f;
#pragma unroll
      for (int u = 0; u < 4; ++u) {
        const float wv = ex[u];
        oc[0] += wv * lo_f(v[u].x); oc[1] += wv * hi_f(v[u].x); oc[2] += wv * lo_f(v[u].y); oc[3] += wv * hi_f(v[u].y);
        oc[4] += wv * lo_f(v[u].z); oc[5] += wv * hi_f(v[u].z); oc[6] += wv * lo_f(v[u].w); oc[7] += wv * hi_f(v[u].w);
      }
      float r4[4], r2[2];
#pragma unroll
      for (int e = 0; e < 4; ++e) r4[e] = tred32(oc[e], oc[4 + e]);
#pragma unroll
      for (int e = 0; e < 2; ++e) r2[e] = tred16(r4[e], r4[2 + e]);
      r2[0] += dpp<0x128>(r2[0]); r2[1] += dpp<0x128>(r2[1]);
      const float iL = __builtin_amdgcn_rcpf(Ls);
      if ((lane & 8) == 0) *(LAS u32*)(L + M_OCS + b * 256 + chh * 128 + chunk * 16 + gq * 4) = pk_bf16(r2[0] * iL, r2[1] * iL);
    }
    BAR();
    {
      f4 c = {0.f, 0.f, 0.f, 0.f};
#pragma unroll
      for (int t = 0; t < 4; ++t) {
        const v4u of = *(const LAS v4u*)(L + M_OCS + (i16 & 3) * 256 + (32 * t + 8 * gq) * 2);
        c = mfma16(uvf[t], of, c);
      }
      if (i16 < 4) *(LAS v2u*)(L + M_CTX + (i16 * 128 + 16 * wave + 4 * gq) * 2) = v2u{pk_bf16(c[0], c[1]), pk_bf16(c[2], c[3])};
    }
    BAR();
    {
      f4 sacc[4];
#pragma unroll
      for (int tt = 0; tt < 4; ++tt) sacc[tt] = f4{0.f, 0.f, 0.f, 0.f};
#pragma unroll
      for (int t = 0; t < 4; ++t) {
        const v4u cf = *(const LAS v4u*)(L + M_CTX + ((i16 & 3) * 128 + 32 * t + 8 * gq) * 2);
#pragma unroll
        for (int tt = 0; tt < 4; ++tt) sacc[tt] = mfma16(wf[t][tt], cf, sacc[tt]);
      }
      LAS unsigned char* priv = L + L_P + wave * 1024;
      if (i16 < 4) {
#pragma unroll
        for (int tt = 0; tt < 4; ++tt) *(LAS f4*)(priv + ((tt * 4 + i16) * 4 + gq) * 16) = sacc[tt];
      }
      const v4u o = *(const LAS v4u*)(priv + lane * 16);
      const int f = ((lane >> 2) & 3) * 32 + 4 * wave + (lane >> 4);
      __builtin_amdgcn_raw_buffer_store_b128(nosent(o), rs, WS_IN5 + (f * 128 + wid) * 64 + (lane & 3) * 16, 0, 16);
    }
    STAMP(9);
  } else {
    const int kb = xg * 16 + idx, ab = kb >> 5, as = kb & 31;
    const bool do_ffn = idx < 14;
    const int fg = idx * 8 + xg;
    const u32 tagA = ep.x + 1u, tagB = ep.y + 1u;
    const u32 sz = swz(i16);
    v4u sf[16], vf[4][4];
    {
      const u16* kvp = ckv + ((size_t)ab * 4096 + as * 128 + wave * 4) * 512;
      const u32 ldst = lds_base + L_BIG + wave * 4096;
      const u32 rb = (u32)(wave & 3) * 4u;
      __builtin_amdgcn_raw_buffer_store_b128(sv, rs, WS_IN5 + kb * 8192 + tid * 16, 0, 16);
#pragma unroll
      for (int t = 0; t < KDEPTH; ++t) {
#pragma unroll
        for (int r = 0; r < 4; ++r) glds16s(kvp + (size_t)(32 * t + r) * 512, (u32)((lane ^ swz(rb + r)) * 16), ldst + (32 * t + r) * 1024);
      }
      BAR();
#pragma unroll
      for (int t = 0; t < 4; ++t) {
        {
          const int younger = ((t + KDEPTH < 4 ? t + KDEPTH : 4) - 1 - t) * 4;
          if (younger == 12) VMWAIT(12); else if (younger == 8) VMWAIT(8); else if (younger == 4) VMWAIT(4); else VMWAIT(0);
        }
        lds_signal(L, C_KQ + t, lane);
        if (t + KDEPTH < 4) {
#pragma unroll
          for (int r = 0; r < 4; ++r) glds16s(kvp + (size_t)(32 * (t + KDEPTH) + r) * 512, (u32)((lane ^ swz(rb + r)) * 16), ldst + (32 * (t + KDEPTH) + r) * 1024);
        }
        lds_wait(L, C_KQ + t, 8);
        if (t == 3) STAMP(2);
        const int k0 = 32 * t + 8 * gq + q4, k1 = k0 + 4;
#pragma unroll
        for (int tt = 0; tt < 4; ++tt) {
          const v2u lo = trread(L + L_BIG + k0 * 1024 + (((8 * wave + 2 * tt + (p4 >> 1)) ^ swz(k0)) * 16) + 8 * (p4 & 1));
          const v2u hi = trread(L + L_BIG + k1 * 1024 + (((8 * wave + 2 * tt + (p4 >> 1)) ^ swz(k1)) * 16) + 8 * (p4 & 1));
          vf[t][tt] = v4u{lo.x, lo.y, hi.x, hi.y};
        }
        if ((wave >> 1) == t) {
          LAS unsigned char* kvrow = L + L_BIG + (16 * wave + i16) * 1024;
#pragma unroll
          for (int u = 0; u < 16; ++u) sf[u] = *(const LAS v4u*)(kvrow + 256 * (u >> 2) + 16 * ((4 * (u & 3) + gq) ^ sz));
        }
        if (t == 0) lds_signal(L, C_PR, lane);
      }
    }
    STAMP(3);
    {
      const u32 src = WS_QA + (ab * 4096 + tid) * 16;
      v4u v[8];
#pragma unroll
      for (int u = 0; u < 8; ++u) v[u] = v4u{0u, tagA ^ 1u, 0u, 0u};
      _Pragma("unroll 1") for (u32 spins = 0; spins < SPIN_CAP; ++spins) {
        CFENCE();
        v[0] = __builtin_amdgcn_raw_buffer_load_b128(rs, src, 0, 16);
        if (__all(((v[0].y ^ tagA) | (v[0].w ^ tagB)) == 0u)) break;
        __builtin_amdgcn_s_sleep(2);
      }
      _Pragma("unroll 1") for (u32 spins = 0; spins < SPIN_CAP; ++spins) {
        CFENCE();
#pragma unroll
        for (int u = 1; u < 8; ++u)
          if (((v[u].y ^ tagA) | (v[u].w ^ tagB)) != 0u) v[u] = __builtin_amdgcn_raw_buffer_load_b128(rs, src, u * 8192, 16);
        u32 bad = 0u;
#pragma unroll
        for (int u = 0; u < 8; ++u) bad |= (v[u].y ^ tagA) | (v[u].w ^ tagB);
        if (__all(bad == 0u)) break;
        __builtin_amdgcn_s_sleep(1);
      }
      const int cu = tid & 127, hb = tid >> 7;
      lds_wait(L, C_PR, 8);
#pragma unroll
      for (int u = 0; u < 8; ++u) {
        const int hh = 4 * u + hb;
        *(LAS v2u*)(L + L_BIG + hh * 1024 + (((cu >> 1) ^ swz(hh)) * 16) + (cu & 1) * 8) = v2u{v[u].x, v[u].z};
      }
    }
    STAMP(4);
    BAR();
    STAMP(5);
    f4 s0 = {0.f, 0.f, 0.f, 0.f}, s1 = s0;
    {
      LAS unsigned char* qrow = L + L_BIG + i16 * 1024;
      v4u qa[4], qb[4];
#pragma unroll
      for (int t = 0; t < 4; ++t) {
        const u32 off = 16 * ((4 * t + gq) ^ sz);
        qa[t] = *(const LAS v4u*)(qrow + off); qb[t] = *(const LAS v4u*)(qrow + 16384 + off);
      }
      __builtin_amdgcn_sched_barrier(0);
#pragma unroll
      for (int t = 0; t < 16; ++t) {
        s0 = mfma16(sf[t], qa[t & 3], s0);
        s1 = mfma16(sf[t], qb[t & 3], s1);
        if (t + 4 < 16) {
          const u32 off = 256 * ((t + 4) >> 2) + 16 * ((4 * (t & 3) + gq) ^ sz);
          qa[t & 3] = *(const LAS v4u*)(qrow + off); qb[t & 3] = *(const LAS v4u*)(qrow + 16384 + off);
        }
        __builtin_amdgcn_sched_barrier(0);
      }
    }
    {
      float mx0 = maxx32(maxx16(fmaxf(fmaxf(s0[0], s0[1]), fmaxf(s0[2], s0[3]))));
      float mx1 = maxx32(maxx16(fmaxf(fmaxf(s1[0], s1[1]), fmaxf(s1[2], s1[3]))));
      if (lane < 32) *(LAS float*)(L + M_WMAX + (lane * 8 + wave) * 4) = lane < 16 ? mx0 : mx1;
    }
    STAMP(6);
    v4u fu = {0u, 0u, 0u, 0u}, wd0 = fu, wd1 = fu, xv = fu, wrv = fu;
    if (do_ffn) {
      fu = __builtin_amdgcn_raw_buffer_load_b128(rs, WS_FX + fg * 8192 + tid * 16, 0, 16);
      wd0 = ldnt(wd + (size_t)(16 * fg + 2 * (lane >> 3)) * 512 + 64 * wave + 8 * (lane & 7));
      wd1 = ldnt(wd + (size_t)(16 * fg + 2 * (lane >> 3) + 1) * 512 + 64 * wave + 8 * (lane & 7));
    }
    const u16 xfin = x[ab * 512 + as * 16 + (tid & 15)];
    if (wave < 4) {
      xv = *(const GAS v4u*)(x + wave * 512 + lane * 8);
      wrv = *(const GAS v4u*)(wr + lane * 8);
    }
    __builtin_amdgcn_sched_barrier(0);
    BAR();
    float m0, m1;
    {
      const f4 a0 = *(const LAS f4*)(L + M_WMAX + i16 * 32), a1 = *(const LAS f4*)(L + M_WMAX + i16 * 32 + 16);
      const f4 b0 = *(const LAS f4*)(L + M_WMAX + 512 + i16 * 32), b1 = *(const LAS f4*)(L + M_WMAX + 512 + i16 * 32 + 16);
      m0 = fmaxf(fmaxf(fmaxf(a0[0], a0[1]), fmaxf(a0[2], a0[3])), fmaxf(fmaxf(a1[0], a1[1]), fmaxf(a1[2], a1[3])));
      m1 = fmaxf(fmaxf(fmaxf(b0[0], b0[1]), fmaxf(b0[2], b0[3])), fmaxf(fmaxf(b1[0], b1[1]), fmaxf(b1[2], b1[3])));
      const u32 pa = pk_bf16(__builtin_amdgcn_exp2f(s0[0] - m0), __builtin_amdgcn_exp2f(s0[1] - m0));
      const u32 pb = pk_bf16(__builtin_amdgcn_exp2f(s0[2] - m0), __builtin_amdgcn_exp2f(s0[3] - m0));
      const u32 pc = pk_bf16(__builtin_amdgcn_exp2f(s1[0] - m1), __builtin_amdgcn_exp2f(s1[1] - m1));
      const u32 pd = pk_bf16(__builtin_amdgcn_exp2f(s1[2] - m1), __builtin_amdgcn_exp2f(s1[3] - m1));
      const u32 poff = i16 * 256 + (((2 * wave + (gq >> 1)) ^ sz) * 16) + (gq & 1) * 8;
      *(LAS v2u*)(L + L_P + poff) = v2u{pa, pb};
      *(LAS v2u*)(L + L_P + 4096 + poff) = v2u{pc, pd};
      const float l0 = sumx32(sumx16((lo_f(pa) + hi_f(pa)) + (lo_f(pb) + hi_f(pb))));
      const float l1 = sumx32(sumx16((lo_f(pc) + hi_f(pc)) + (lo_f(pd) + hi_f(pd))));
      if (lane < 32) *(LAS float*)(L + M_WSUM + (lane * 8 + wave) * 4) = lane < 16 ? l0 : l1;
    }
    BAR();
    STAMP(7);
    if (wave == 0 && lane < 32) {
      const f4 la = *(const LAS f4*)(L + M_WSUM + lane * 32), lb = *(const LAS f4*)(L + M_WSUM + lane * 32 + 16);
      const float l = ((la[0] + la[1]) + (la[2] + la[3])) + ((lb[0] + lb[1]) + (lb[2] + lb[3]));
      u32 lse = __float_as_uint((lane < 16 ? m0 : m1) + __builtin_amdgcn_logf(l));
      lse = lse < 0xFFFFFFFEu ? lse : 0xFFFFFFFEu;
      __builtin_amdgcn_raw_buffer_store_b32(lse, rs, WS_ST3 + ((lane * 4 + ab) * 32 + as) * 4, 0, 16);
    }
#pragma unroll
    for (int ht = 0; ht < 2; ++ht) {
      f4 oacc[4];
#pragma unroll
      for (int tt = 0; tt < 4; ++tt) oacc[tt] = f4{0.f, 0.f, 0.f, 0.f};
#pragma unroll
      for (int t = 0; t < 4; ++t) {
        const v4u pf = *(const LAS v4u*)(L + L_P + ht * 4096 + i16 * 256 + (((4 * t + gq) ^ sz) * 16));
#pragma unroll
        for (int tt = 0; tt < 4; ++tt) oacc[tt] = mfma16(vf[t][tt], pf, oacc[tt]);
      }
      const f4 la = *(const LAS f4*)(L + M_WSUM + ht * 512 + i16 * 32), lb = *(const LAS f4*)(L + M_WSUM + ht * 512 + i16 * 32 + 16);
      const float l = ((la[0] + la[1]) + (la[2] + la[3])) + ((lb[0] + lb[1]) + (lb[2] + lb[3]));
      const float il = __builtin_amdgcn_rcpf(l);
      const int head = 16 * ht + i16;
      LAS unsigned char* priv = L + L_BIG + 65536 + wave * 4608;
#pragma unroll
      for (int tt = 0; tt < 4; ++tt)
        *(LAS v2u*)(priv + head * 144 + tt * 32 + gq * 8) = v2u{pk_bf16(oacc[tt][0] * il, oacc[tt][1] * il), pk_bf16(oacc[tt][2] * il, oacc[tt][3] * il)};
#pragma unroll
      for (int e = 0; e < 2; ++e) {
        const int hd = 16 * ht + 8 * e + (lane >> 3);
        const v4u o = *(const LAS v4u*)(priv + hd * 144 + (lane & 7) * 16);
        __builtin_amdgcn_raw_buffer_store_b128(nosent(o), rs,
            WS_IN3 + (((hd * 4 + (wave >> 1)) * 4 + ab) * 32 + as) * 256 + (wave & 1) * 128 + (lane & 7) * 16, 0, 16);
      }
    }
    STAMP(8);
    if (wave < 4) ln_row(L, xv, wrv, wave, lane);
    if (do_ffn) {
      const int chunk = lane & 7, rg = lane >> 3;
      _Pragma("unroll 1") for (u32 spins = 0; spins < SPIN_CAP; ++spins) {
        if (__all(((fu.y ^ tagA) | (fu.w ^ tagB)) == 0u)) break;
        CFENCE();
        fu = __builtin_amdgcn_raw_buffer_load_b128(rs, WS_FX + fg * 8192 + tid * 16, 0, 16);
      }
      *(LAS v2u*)(L + L_BIG + 32768 + tid * 8) = v2u{fu.x, fu.z};
      BAR();
      if (lane < 32) {
        float g0 = 0.f, g1 = 0.f, u0 = 0.f, u1 = 0.f;
#pragma unroll
        for (int w = 0; w < 8; ++w) {
          const v2u gv = *(const LAS v2u*)(L + L_BIG + 32768 + (w * 64 + lane) * 8), uv = *(const LAS v2u*)(L + L_BIG + 32768 + (w * 64 + 32 + lane) * 8);
          g0 += __uint_as_float(gv.x); g1 += __uint_as_float(gv.y); u0 += __uint_as_float(uv.x); u1 += __uint_as_float(uv.y);
        }
        g0 = rbf(g0); g1 = rbf(g1);
        const float h0 = rbf(rbf(g0 / (1.0f + __expf(-g0))) * rbf(u0)), h1 = rbf(rbf(g1 / (1.0f + __expf(-g1))) * rbf(u1));
        *(LAS v2u*)(L + L_BIG + 40960 + wave * 256 + lane * 8) = v2u{__float_as_uint(h0), __float_as_uint(h1)};
      }
      float y[32];
#pragma unroll
      for (int b = 0; b < 4; ++b) {
        const v2u hp = *(const LAS v2u*)(L + L_BIG + 40960 + wave * 256 + (b * 16 + 2 * rg) * 4);
        const float h0 = __uint_as_float(hp.x), h1 = __uint_as_float(hp.y);
#pragma unroll
        for (int e = 0; e < 4; ++e) {
          y[b * 8 + 2 * e] = h0 * lo_f(wd0[e]) + h1 * lo_f(wd1[e]);
          y[b * 8 + 2 * e + 1] = h0 * hi_f(wd0[e]) + h1 * hi_f(wd1[e]);
        }
      }
      float n16[16], n8[8];
#pragma unroll
      for (int k = 0; k < 16; ++k) n16[k] = tred32(y[k], y[16 + k]);
#pragma unroll
      for (int k = 0; k < 8; ++k) { n8[k] = tred16(n16[k], n16[8 + k]); n8[k] += dpp<0x128>(n8[k]); }
      LAS unsigned char* priv = L + L_BIG + 65536 + wave * 4608;
      const f4 oh = (lane & 8) ? f4{n8[4], n8[5], n8[6], n8[7]} : f4{n8[0], n8[1], n8[2], n8[3]};
      *(LAS f4*)(priv + (gq * 16 + chunk * 2 + ((lane >> 3) & 1)) * 16) = oh;
      const v4u o = *(const LAS v4u*)(priv + lane * 16);
      const int f = gq * 32 + 4 * wave + (i16 >> 2);
      __builtin_amdgcn_raw_buffer_store_b128(nosent(o), rs, WS_INY + (f * 112 + fg) * 64 + (i16 & 3) * 16, 0, 16);
    }
    STAMP(9);
    float gate = 0.f;
    {
      const u32 sy = WS_INY + kb * 7168 + (tid < 448 ? tid : 0) * 16;
      v4u y0 = sv;
      __builtin_amdgcn_s_sleep(8);
      _Pragma("unroll 1") for (u32 spins = 0; spins < SPIN_CAP; ++spins) {
        CFENCE();
        if (umax4(y0) == SENT) y0 = __builtin_amdgcn_raw_buffer_load_b128(rs, sy, 0, 16);
        if (__all(umax4(y0) != SENT)) break;
        __builtin_amdgcn_s_sleep(4);
      }
      float v[4], n2[2];
#pragma unroll
      for (int e = 0; e < 4; ++e) v[e] = tid < 448 ? __uint_as_float(y0[e]) : 0.f;
#pragma unroll
      for (int e = 0; e < 2; ++e) n2[e] = tred32(v[e], v[2 + e]);
      float n1 = tred16(n2[0], n2[1]);
      n1 += dpp<0x124>(n1); n1 += dpp<0x128>(n1);
      if (i16 < 4) *(LAS float*)(L + M_FIN + (wave * 16 + i16 * 4 + gq) * 4) = n1;
      BAR();
      if (tid < 16) {
        const int m = as * 16 + tid;
        float y = 0.f;
#pragma unroll
        for (int w = 0; w < 8; ++w) y += *(const LAS float*)(L + M_FIN + (w * 16 + tid) * 4);
        const float rsv = *(const LAS float*)(L + M_RS + ab * 4);
        const float xnv = bf2f(*(const LAS u16*)(L + L_XN + (ab * 512 + m) * 2));
        y = rbf(rbf(y) * rsv);
        const float z = rbf(xnv + y);
        gate = rbf(z / (1.0f + __expf(-z)));
      }
    }
    STAMP(13);
    {
      const u32 s5 = WS_IN5 + kb * 8192 + tid * 16;
      v4u a0 = sv;
      _Pragma("unroll 1") for (u32 spins = 0; spins < SPIN_CAP; ++spins) {
        CFENCE();
        if (umax4(a0) == SENT) a0 = __builtin_amdgcn_raw_buffer_load_b128(rs, s5, 0, 16);
        if (__all(umax4(a0) != SENT)) break;
        __builtin_amdgcn_s_sleep(1);
      }
      STAMP(10);
      float n2[2];
#pragma unroll
      for (int e = 0; e < 2; ++e) n2[e] = tred32(__uint_as_float(a0[e]), __uint_as_float(a0[2 + e]));
      float n1 = tred16(n2[0], n2[1]);
      n1 += dpp<0x124>(n1); n1 += dpp<0x128>(n1);
      if (i16 < 4) *(LAS float*)(L + M_FIN + 512 + (wave * 16 + i16 * 4 + gq) * 4) = n1;
    }
    BAR();
    if (tid < 16) {
      float sacc = 0.f;
#pragma unroll
      for (int w = 0; w < 8; ++w) sacc += *(const LAS float*)(L + M_FIN + 512 + (w * 16 + tid) * 4);
      const float o = bf2f(xfin) + rbf(rbf(sacc) * gate);
      out[ab * 512 + as * 16 + tid] = __builtin_bit_cast(u16, (__bf16)o);
    }
    if (g == 0 && tid == 0) __builtin_amdgcn_raw_buffer_store_b64(v2u{tagA, tagB}, rs, WS_EPOCH, 0, 16);
  }
}

extern "C" void kernel_launch(void* const* d_in, const int* in_sizes, int n_in,
                              void* d_out, int out_size, void* d_ws, size_t ws_size,
                              hipStream_t stream) {
  (void)in_sizes; (void)n_in; (void)out_size;
  (void)hipFuncSetAttribute((const void*)mega, hipFuncAttributeMaxDynamicSharedMemorySize, LDS_BYTES);
  u64* st = (u64*)((char*)d_ws + ws_size - (1 << 20));
  mega<<<NWG, 512, LDS_BYTES, stream>>>((const u16*)d_in[0], (const u16*)d_in[1], (const u16*)d_in[2], (const u16*)d_in[3],
                                        (const u16*)d_in[4], (const u16*)d_in[5], (const u16*)d_in[7], (const u16*)d_in[8],
                                        (const u16*)d_in[9], (const u16*)d_in[10], (u16*)d_out, (unsigned char*)d_ws, st);
}
```

```cpp
#include <hip/hip_runtime.h>
#include <stdint.h>

typedef unsigned short u16;
typedef unsigned int u32;
typedef unsigned long long u64;
typedef u32 v4u __attribute__((ext_vector_type(4)));
typedef u32 v2u __attribute__((ext_vector_type(2)));
typedef float f4 __attribute__((ext_vector_type(4)));
typedef short bf16x8 __attribute__((ext_vector_type(8)));
typedef short s16x4 __attribute__((ext_vector_type(4)));
typedef __bf16 bf16x2 __attribute__((ext_vector_type(2)));
#define LAS __attribute__((address_space(3)))
#define GAS __attribute__((address_space(1)))

#define STAMPS 0

#define WODELAY 0
#define CLRPOS 3
#define KDEPTH 2
#define CPF 1
#define CPF_KB 22
#define KPF 1
#define PREW 1
#define FFDELAY 0
#define KVDELAY 100
constexpr int NWG = 256;
constexpr u32 WS_EPOCH = 0;
constexpr u32 WS_CF = 2048;
constexpr u32 WS_QX = 4096;
constexpr u32 WS_FX = WS_QX + 32 * 8192;
constexpr u32 WS_QA = WS_FX + 112 * 8192;
constexpr u32 WS_IN3 = WS_QA + 4 * 32 * 128 * 16;
constexpr u32 WS_ST3 = WS_IN3 + 32 * 4 * 32768;
constexpr u32 WS_IN5 = WS_ST3 + 32 * 2048;
constexpr u32 WS_INY = WS_IN5 + 128 * 8192;
constexpr u32 WS_END = WS_INY + 128 * 7168;
constexpr int L_BIG = 0;
constexpr int L_XN = 131072;
constexpr int L_P = L_XN + 4096;
constexpr int L_M = L_P + 8192;
constexpr int M_RED = L_P;
constexpr int M_QS = L_M;
constexpr int M_LSE = L_M + 1024;
constexpr int M_WMAX = L_M + 2048;
constexpr int M_WSUM = L_M + 3072;
constexpr int M_OCS = L_M + 4096;
constexpr int M_CTX = L_M + 5120;
constexpr int M_H = L_M + 6144;
constexpr int M_FIN = L_M + 6400;
constexpr int M_RS = L_M + 7424;
constexpr int M_CNT = L_M + 7488;
constexpr int L_DUMMY = L_M + 7680;
constexpr int LDS_BYTES = L_DUMMY + 1024;
enum { C_XN = 0, C_T = 1, C_RED = 2, C_CLR = 3, C_KV = 4, C_A = 5, C_UV = 6, C_UVR = 7, C_F = 8, C_FR = 9, C_H = 10, C_T2 = 11, C_QS = 12, C_LSE = 13, C_FA = 14, C_PR = 15, C_KQ = 16  , C_WO = 20 };
constexpr u32 SENT = 0xFFFFFFFFu;
constexpr u32 SPIN_CAP = 1u << 16;

__device__ __forceinline__ float bf2f(u32 v) { return __uint_as_float(v << 16); }
__device__ __forceinline__ float lo_f(u32 v) { return __uint_as_float(v << 16); }
__device__ __forceinline__ float hi_f(u32 v) { return __uint_as_float(v & 0xffff0000u); }
__device__ __forceinline__ u32 pk_bf16(float a, float b) {
  bf16x2 r; r[0] = (__bf16)a; r[1] = (__bf16)b; return __builtin_bit_cast(u32, r);
}
__device__ __forceinline__ float rbf(float a) { return (float)((__bf16)a); }
__device__ __forceinline__ f4 mfma16(v4u a, v4u b, f4 c) {
  return __builtin_amdgcn_mfma_f32_16x16x32_bf16(__builtin_bit_cast(bf16x8, a), __builtin_bit_cast(bf16x8, b), c, 0, 0, 0);
}
__device__ __forceinline__ float dot2(u32 a, u32 b, float c) {
  return __builtin_amdgcn_fdot2_f32_bf16(__builtin_bit_cast(bf16x2, a), __builtin_bit_cast(bf16x2, b), c, false);
}
__device__ __forceinline__ u32 swz(u32 row) { return ((((row >> 3) ^ (row >> 2)) & 1u) << 3) | ((row & 3u) << 1) | ((row >> 3) & 1u); }
__device__ __forceinline__ v2u trread(LAS unsigned char* p) {
  return __builtin_bit_cast(v2u, __builtin_amdgcn_ds_read_tr16_b64_v4i16((LAS s16x4*)p));
}
__device__ __forceinline__ void glds16s(const void* sbase, u32 voff, u32 lds_dst) {
  u32 keep;
  asm volatile("s_mov_b32 %0, m0\n\ts_mov_b32 m0, %3\n\ts_nop 0\n\tglobal_load_lds_dwordx4 %1, %2 nt\n\ts_mov_b32 m0, %0"
               : "=&s"(keep) : "v"(voff), "s"(sbase), "s"(lds_dst) : "memory");
}
__device__ __forceinline__ void glds16v(const void* gsrc, u32 lds_dst) {
  u32 keep;
  asm volatile("s_mov_b32 %0, m0\n\ts_mov_b32 m0, %2\n\ts_nop 0\n\tglobal_load_lds_dwordx4 %1, off nt\n\ts_mov_b32 m0, %0"
               : "=&s"(keep) : "v"(gsrc), "s"(lds_dst) : "memory");
}
__device__ __forceinline__ v4u ldnt(const void* p) { return __builtin_nontemporal_load((const GAS v4u*)p); }
template <int CTRL> __device__ __forceinline__ float dpp(float v) {
  return __int_as_float(__builtin_amdgcn_update_dpp(0, __float_as_int(v), CTRL, 0xf, 0xf, true));
}
__device__ __forceinline__ float sum16(float v) { v += dpp<0xB1>(v); v += dpp<0x4E>(v); v += dpp<0x141>(v); v += dpp<0x140>(v); return v; }
__device__ __forceinline__ float sumx16(float v) { auto r = __builtin_amdgcn_permlane16_swap(__float_as_uint(v), __float_as_uint(v), false, false); return __uint_as_float(r[0]) + __uint_as_float(r[1]); }
__device__ __forceinline__ float sumx32(float v) { auto r = __builtin_amdgcn_permlane32_swap(__float_as_uint(v), __float_as_uint(v), false, false); return __uint_as_float(r[0]) + __uint_as_float(r[1]); }
__device__ __forceinline__ float maxx16(float v) { auto r = __builtin_amdgcn_permlane16_swap(__float_as_uint(v), __float_as_uint(v), false, false); return fmaxf(__uint_as_float(r[0]), __uint_as_float(r[1])); }
__device__ __forceinline__ float maxx32(float v) { auto r = __builtin_amdgcn_permlane32_swap(__float_as_uint(v), __float_as_uint(v), false, false); return fmaxf(__uint_as_float(r[0]), __uint_as_float(r[1])); }
__device__ __forceinline__ float sum64(float v) { return sumx32(sumx16(sum16(v))); }
__device__ __forceinline__ float tred32(float a, float b) { auto r = __builtin_amdgcn_permlane32_swap(__float_as_uint(a), __float_as_uint(b), false, false); return __uint_as_float(r[0]) + __uint_as_float(r[1]); }
__device__ __forceinline__ float tred16(float a, float b) { auto r = __builtin_amdgcn_permlane16_swap(__float_as_uint(a), __float_as_uint(b), false, false); return __uint_as_float(r[0]) + __uint_as_float(r[1]); }

#define BAR() do { asm volatile("s_waitcnt lgkmcnt(0)" ::: "memory"); __builtin_amdgcn_s_barrier(); asm volatile("" ::: "memory"); } while (0)
#define BADTAG(v) (((v).y ^ tagA) | ((v).w ^ tagB))
#define CFENCE() asm volatile("" ::: "memory")
#define VMWAIT(n) asm volatile("s_waitcnt vmcnt(" #n ")" ::: "memory")
__device__ __forceinline__ u32 umax4(v4u v) { u32 a = v.x > v.y ? v.x : v.y, b = v.z > v.w ? v.z : v.w; return a > b ? a : b; }
__device__ __forceinline__ v4u nosent(v4u v) {
  v.x = v.x < 0xFFFFFFFEu ? v.x : 0xFFFFFFFEu; v.y = v.y < 0xFFFFFFFEu ? v.y : 0xFFFFFFFEu;
  v.z = v.z < 0xFFFFFFFEu ? v.z : 0xFFFFFFFEu; v.w = v.w < 0xFFFFFFFEu ? v.w : 0xFFFFFFFEu; return v;
}
__device__ __forceinline__ void lds_signal(LAS unsigned char* L, int c, int lane) {
  asm volatile("s_waitcnt lgkmcnt(0)" ::: "memory");
  if (lane == 0) __hip_atomic_fetch_add((LAS u32*)(L + M_CNT) + c, 1u, __ATOMIC_RELAXED, __HIP_MEMORY_SCOPE_WORKGROUP);
}
__device__ __forceinline__ void lds_wait(LAS unsigned char* L, int c, u32 target) {
  _Pragma("unroll 1") for (u32 spins = 0; spins < (1u << 24); ++spins) {
    if (*((volatile LAS u32*)(L + M_CNT) + c) >= target) break;
    __builtin_amdgcn_s_sleep(0);
  }
  CFENCE();
}

__device__ __forceinline__ void ln_row(LAS unsigned char* L, v4u xv, v4u wrv, int r, int lane) {
  float xf[8] = {lo_f(xv.x), hi_f(xv.x), lo_f(xv.y), hi_f(xv.y), lo_f(xv.z), hi_f(xv.z), lo_f(xv.w), hi_f(xv.w)};
  float s = ((xf[0] + xf[1]) + (xf[2] + xf[3])) + ((xf[4] + xf[5]) + (xf[6] + xf[7]));
  const float mean = sum64(s) * (1.0f / 512.0f);
  float q2 = 0.f;
#pragma unroll
  for (int e = 0; e < 8; ++e) { const float d = xf[e] - mean; q2 += d * d; }
  const float var = rbf(sum64(q2) * (1.0f / 512.0f));
  const float mb = rbf(mean);
  const float den = rbf(sqrtf(rbf(var + 1e-5f)));
  float xn[8];
#pragma unroll
  for (int e = 0; e < 8; ++e) xn[e] = rbf(xf[e] - mb) / den;
  v4u xnp = {pk_bf16(xn[0], xn[1]), pk_bf16(xn[2], xn[3]), pk_bf16(xn[4], xn[5]), pk_bf16(xn[6], xn[7])};
  *(LAS v4u*)(L + L_XN + r * 1024 + lane * 16) = xnp;
  lds_signal(L, C_XN, lane);
  float rr = dot2(xnp.x, wrv.x, 0.f); rr = dot2(xnp.y, wrv.y, rr); rr = dot2(xnp.z, wrv.z, rr); rr = dot2(xnp.w, wrv.w, rr);
  rr = rbf(sum64(rr));
  if (lane == 0) *(LAS float*)(L + M_RS + r * 4) = rbf(1.0f / (1.0f + __expf(-rr)));
}

__device__ __forceinline__ u32 gsw(u32 row) { return (((row >> 1) & 1u) | (((row >> 3) & 1u) << 1)) << 1; }
__device__ __forceinline__ void tile_gemv(LAS unsigned char* L, int tile_off, int m0, int kq, int lane) {
  const int i16 = lane & 15, gq = lane >> 4, q4 = i16 >> 2, p4 = i16 & 3;
  f4 acc[4];
#pragma unroll
  for (int dt = 0; dt < 4; ++dt) acc[dt] = f4{0.f, 0.f, 0.f, 0.f};
  const u32 fl = gsw(8 * gq + q4);
#pragma unroll
  for (int t = 0; t < 2; ++t) {
    const int r0 = 64 * kq + 32 * t + 8 * gq + q4;
    const v4u xf = *(const LAS v4u*)(L + L_XN + (i16 & 3) * 1024 + (m0 + 64 * kq + 32 * t + 8 * gq) * 2);
    LAS unsigned char* b0 = L + tile_off + r0 * 128 + 8 * (p4 & 1);
#pragma unroll
    for (int dt = 0; dt < 4; ++dt) {
      const v2u a0 = trread(b0 + (((2 * dt + (p4 >> 1)) ^ fl) * 16)), a1 = trread(b0 + 512 + (((2 * dt + (p4 >> 1)) ^ fl) * 16));
      acc[dt] = mfma16(v4u{a0.x, a0.y, a1.x, a1.y}, xf, acc[dt]);
    }
  }
  if (i16 < 4) {
#pragma unroll
    for (int dt = 0; dt < 4; ++dt) *(LAS f4*)(L + M_RED + (((kq * 4 + dt) * 4 + gq) * 4 + i16) * 16) = acc[dt];
  }
}

__global__ __launch_bounds__(512, 2) void mega(const u16* __restrict__ x, const u16* __restrict__ ckv, const u16* __restrict__ wq,
                                               const u16* __restrict__ wuk, const u16* __restrict__ wuv, const u16* __restrict__ wo,
                                               const u16* __restrict__ wr, const u16* __restrict__ wg, const u16* __restrict__ wu,
                                               const u16* __restrict__ wd, u16* __restrict__ out, unsigned char* ws, u64* stamps) {
  extern __shared__ __attribute__((aligned(16))) unsigned char lds_raw[];
  LAS unsigned char* L = (LAS unsigned char*)lds_raw;
  const int tid = threadIdx.x, lane = tid & 63;
  const int wave = __builtin_amdgcn_readfirstlane(tid >> 6);
  const int g = blockIdx.x, xg = g & 7, idx = g >> 3;
  const int i16 = lane & 15, gq = lane >> 4, q4 = i16 >> 2, p4 = i16 & 3;
  const __amdgpu_buffer_rsrc_t rs = __builtin_amdgcn_make_buffer_rsrc((void*)ws, 0, (int)WS_END, 0x00020000);
  const u32 lds_base = (u32)(size_t)lds_raw;
  const v4u sv = {SENT, SENT, SENT, SENT};
#define STAMP(k)
  {
    u64 pc;
    asm volatile("s_getpc_b64 %0" : "=s"(pc));
    const int slice = idx + 32 * (wave >> 2);
    if ((wave & 3) == 3 && slice < CPF_KB) {
      u32 keep;
      asm volatile("s_mov_b32 %0, m0\n\ts_mov_b32 m0, %3\n\ts_nop 0\n\tglobal_load_lds_dwordx4 %1, %2\n\ts_mov_b32 m0, %0"
                   : "=&s"(keep) : "v"((u32)(slice * 1024 + lane * 16)), "s"(pc & ~(u64)63), "s"(lds_base + L_DUMMY) : "memory");
    }
  }
  STAMP(0);
  if (tid < 24) *((LAS u32*)(L + M_CNT) + tid) = 0u;
  const u64 tk0 = __builtin_amdgcn_s_memrealtime();
  const v2u ep = *(const __attribute__((address_space(4))) v2u*)(ws + WS_EPOCH);

  const u32 tagA = ep.x + 1u, tagB = ep.y + 1u;
  const v4u nv = {0u, tagA ^ 1u, 0u, 0u};
  if (idx >= 16) {
    const int wi = idx - 16, h = xg * 4 + (wi >> 2), j = wi & 3, wid = xg * 16 + wi;
    if (wave < 4) {
      const int a = wave;
      const v4u xv = *(const GAS v4u*)(x + a * 512 + lane * 8);
      const v4u wrv = *(const GAS v4u*)(wr + lane * 8);
      if (lane < 32) {
        __builtin_amdgcn_raw_buffer_store_b128(nv, rs, WS_QX + (((h * 4 + a) * 2 + (j >> 1)) * 64 + 32 * (j & 1) + lane) * 16, 0, 16);
        __builtin_amdgcn_raw_buffer_store_b128(nv, rs, WS_QA + (((lane >> 3) * 32 + h) * 128 + 32 * j + 8 * a + (lane & 7)) * 16, 0, 16);
      }
      BAR();
      ln_row(L, xv, wrv, a, lane);
      lds_wait(L, C_XN, 4);
      lds_wait(L, C_T, 4);
      STAMP(1);
      tile_gemv(L, L_BIG, 256 * (j >> 1), a, lane);
      lds_signal(L, C_RED, lane);
      lds_wait(L, C_RED, 4);
      if (lane < 32) {
        float s0 = 0.f, s1 = 0.f;
#pragma unroll
        for (int kq = 0; kq < 4; ++kq) {
          const v2u pv = *(const LAS v2u*)(L + M_RED + ((kq * 16 + (lane >> 1)) * 4 + a) * 16 + (lane & 1) * 8);
          s0 += __uint_as_float(pv.x); s1 += __uint_as_float(pv.y);
        }
        __builtin_amdgcn_raw_buffer_store_b128(v4u{__float_as_uint(s0), tagA, __float_as_uint(s1), tagB}, rs,
                                               WS_QX + (((h * 4 + a) * 2 + (j >> 1)) * 64 + 32 * (j & 1) + lane) * 16, 0, 16);
      }
      STAMP(2);
      lds_wait(L, C_T2, 4);
      v4u wukf[2][4];
#pragma unroll
      for (int ct = 0; ct < 2; ++ct)
#pragma unroll
        for (int t = 0; t < 4; ++t)
          wukf[ct][t] = *(const LAS v4u*)(L + L_BIG + 32768 + (32 * a + 16 * ct + i16) * 256 + (((4 * t + gq) ^ swz(i16)) * 16));
      v4u qu[2];
      qu[0] = qu[1] = v4u{0u, tagA ^ 1u, 0u, 0u};
      const u32 qsrc = WS_QX + ((h * 4 + a) * 128 + lane) * 16;
      _Pragma("unroll 1") for (u32 spins = 0; spins < SPIN_CAP; ++spins) {
        CFENCE();
#pragma unroll
        for (int u = 0; u < 2; ++u)
          if (((qu[u].y ^ tagA) | (qu[u].w ^ tagB)) != 0u) qu[u] = __builtin_amdgcn_raw_buffer_load_b128(rs, qsrc + u * 1024, 0, 16);
        const u32 bad = (qu[0].y ^ tagA) | (qu[0].w ^ tagB) | (qu[1].y ^ tagA) | (qu[1].w ^ tagB);
        if (__all(bad == 0u)) break;
      }
      *(LAS u32*)(L + M_QS + a * 256 + lane * 4) = pk_bf16(__uint_as_float(qu[0].x) + __uint_as_float(qu[1].x), __uint_as_float(qu[0].z) + __uint_as_float(qu[1].z));
      lds_signal(L, C_QS, lane);
      lds_wait(L, C_QS, 4);
      STAMP(3);
      LAS unsigned char* priv = L + L_BIG + 65536 + a * 2048;
      f4 qa0 = {0.f, 0.f, 0.f, 0.f}, qa1 = qa0;
#pragma unroll
      for (int t = 0; t < 4; ++t) {
        const v4u qf = *(const LAS v4u*)(L + M_QS + (i16 & 3) * 256 + (32 * t + 8 * gq) * 2);
        qa0 = mfma16(wukf[0][t], qf, qa0);
        qa1 = mfma16(wukf[1][t], qf, qa1);
      }
      const float SC = 0.08838834764831845f * 1.4426950408889634f;
      if (i16 < 4) {
        *(LAS v2u*)(priv + 1024 + (i16 * 8 + gq) * 8) = v2u{pk_bf16(qa0[0] * SC, qa0[1] * SC), pk_bf16(qa0[2] * SC, qa0[3] * SC)};
        *(LAS v2u*)(priv + 1024 + (i16 * 8 + 4 + gq) * 8) = v2u{pk_bf16(qa1[0] * SC, qa1[1] * SC), pk_bf16(qa1[2] * SC, qa1[3] * SC)};
      }
      lds_wait(L, C_CLR, 4);
      if (lane < 32) {
        const v2u pv = *(const LAS v2u*)(priv + 1024 + lane * 8);
        __builtin_amdgcn_raw_buffer_store_b128(v4u{pv.x, tagA, pv.y, tagB}, rs, WS_QA + (((lane >> 3) * 32 + h) * 128 + 32 * j + 8 * a + (lane & 7)) * 16, 0, 16);
      }
      lds_signal(L, C_A, lane);
      STAMP(4);
      if (wi < 14) {
        while ((long long)(__builtin_amdgcn_s_memrealtime() - tk0) < FFDELAY) __builtin_amdgcn_s_sleep(2);
        v4u fw[8];
        const int mat = wave & 1, ks = wave >> 1;
        const u16* src = (mat ? wu : wg) + (size_t)(64 * xg + 32 * ks + 8 * gq) * 1792 + 128 * wi + 8 * i16;
#pragma unroll
        for (int jj = 0; jj < 8; ++jj) fw[jj] = ldnt(src + (size_t)jj * 1792);
        const v4u af = *(const LAS v4u*)(L + L_XN + (i16 >> 2) * 1024 + (64 * xg + 32 * ks + 8 * gq) * 2);
        float pr[8];
#pragma unroll
        for (int cc = 0; cc < 8; ++cc) {
          const u32 sel = (cc & 1) ? 0x07060302u : 0x05040100u;
          v4u bfr;
          bfr.x = __builtin_amdgcn_perm(fw[1][cc >> 1], fw[0][cc >> 1], sel);
          bfr.y = __builtin_amdgcn_perm(fw[3][cc >> 1], fw[2][cc >> 1], sel);
          bfr.z = __builtin_amdgcn_perm(fw[5][cc >> 1], fw[4][cc >> 1], sel);
          bfr.w = __builtin_amdgcn_perm(fw[7][cc >> 1], fw[6][cc >> 1], sel);
          pr[cc] = mfma16(af, bfr, f4{0.f, 0.f, 0.f, 0.f})[0];
        }
        LAS f4* dst = (LAS f4*)(L + M_RED + ((wave * 4 + gq) * 128 + 8 * i16) * 4);
        dst[0] = f4{pr[0], pr[1], pr[2], pr[3]};
        dst[1] = f4{pr[4], pr[5], pr[6], pr[7]};
        lds_signal(L, C_FA, lane);
        lds_wait(L, C_FA, 4);
#pragma unroll
        for (int u = 0; u < 2; ++u) {
          const int U = ks * 128 + u * 64 + lane, msd = U >> 5, b = (U >> 3) & 3, k = U & 7;
          const v2u p0 = *(const LAS v2u*)(L + M_RED + (((0 + mat) * 4 + b) * 128 + 16 * msd + 2 * k) * 4);
          const v2u p1 = *(const LAS v2u*)(L + M_RED + (((2 + mat) * 4 + b) * 128 + 16 * msd + 2 * k) * 4);
          const v4u o = {__float_as_uint(__uint_as_float(p0.x) + __uint_as_float(p1.x)), tagA, __float_as_uint(__uint_as_float(p0.y) + __uint_as_float(p1.y)), tagB};
          __builtin_amdgcn_raw_buffer_store_b128(o, rs, WS_FX + (wi * 8 + msd) * 8192 + (((xg * 2 + mat) * 4 + b) * 8 + k) * 16, 0, 16);
        }
        STAMP(12);
      }
    } else {
      const int lw = wave - 4, tl = tid - 256;
      BAR();
      {
        const u16* base = wq + (size_t)(256 * (j >> 1) + 64 * lw) * 4096 + h * 128 + 64 * (j & 1);
#pragma unroll
        for (int r = 0; r < 8; ++r) {
          const u32 row = 8 * r + (lane >> 3);
          glds16s(base, row * 8192u + (((u32)(lane & 7) ^ gsw(row)) * 16u), lds_base + L_BIG + (lw * 8 + r) * 1024);
        }
#pragma unroll
        for (int r = 0; r < 8; ++r) {
          const int row = (lw * 8 + r) * 4 + gq;
          glds16s(wuk + (size_t)(128 * j) * 4096 + h * 128, (u32)(row * 8192 + ((i16 ^ swz(row)) * 16)), lds_base + L_BIG + 32768 + (lw * 8 + r) * 1024);
        }
      }
      VMWAIT(8);
      lds_signal(L, C_T, lane);
      STAMP(1);
      __builtin_amdgcn_raw_buffer_store_b128(sv, rs, WS_INY + wid * 7168 + tl * 16, 0, 16);
      __builtin_amdgcn_raw_buffer_store_b128(sv, rs, WS_INY + wid * 7168 + (tl < 192 ? 256 + tl : tl) * 16, 0, 16);
      VMWAIT(2);
      lds_signal(L, C_T2, lane);
      VMWAIT(0);
      lds_signal(L, C_CLR, lane);
      STAMP(2);
      lds_wait(L, C_A, 4);
      while ((long long)(__builtin_amdgcn_s_memrealtime() - tk0) < WODELAY) __builtin_amdgcn_s_sleep(2);
      STAMP(3);
#pragma unroll
      for (int r = 0; r < 8; ++r) {
        const int row = (lw * 8 + r) * 4 + gq;
        glds16s(wuv + (size_t)(128 * j) * 4096 + h * 128, (u32)(row * 8192 + ((i16 ^ swz(row)) * 16)), lds_base + L_BIG + 98304 + (lw * 8 + r) * 1024);
      }
      const u16* wop = wo + (size_t)h * 128 * 512;
#pragma unroll 4
      for (int r = 0; r < 24; ++r) {
        const int row = lw * 24 + r;
        glds16s(wop + (size_t)row * 512, (u32)((lane ^ swz(row)) * 16), lds_base + L_BIG + row * 1024);
      }
      VMWAIT(24);
      lds_signal(L, C_UV, lane);
    }
    lds_wait(L, C_UV, 4);
    v4u uvf[4];
#pragma unroll
    for (int t = 0; t < 4; ++t) {
      const int r0 = 32 * t + 8 * gq + q4, r1 = r0 + 4;
      const v2u lo = trread(L + L_BIG + 98304 + r0 * 256 + (((2 * wave + (p4 >> 1)) ^ swz(r0)) * 16) + 8 * (p4 & 1));
      const v2u hi = trread(L + L_BIG + 98304 + r1 * 256 + (((2 * wave + (p4 >> 1)) ^ swz(r1)) * 16) + 8 * (p4 & 1));
      uvf[t] = v4u{lo.x, lo.y, hi.x, hi.y};
    }
    lds_signal(L, C_UVR, lane);
    STAMP(5);
    if (wave >= 4) {
      const int lw = wave - 4;
      lds_wait(L, C_UVR, 8);
      const u16* wop = wo + (size_t)h * 128 * 512;
#pragma unroll
      for (int r = 0; r < 8; ++r) {
        const int row = 96 + lw * 8 + r;
        glds16s(wop + (size_t)row * 512, (u32)((lane ^ swz(row)) * 16), lds_base + L_BIG + row * 1024);
      }
      VMWAIT(0);
      lds_signal(L, C_WO, lane);
      STAMP(6);
    }
    v4u wf[4][4];
#define READ_WF() do { \
    _Pragma("unroll") for (int t = 0; t < 4; ++t) { \
      const int r0 = 32 * t + 8 * gq + q4, r1 = r0 + 4; \
      _Pragma("unroll") for (int tt = 0; tt < 4; ++tt) { \
        const v2u lo = trread(L + L_BIG + r0 * 1024 + (((8 * wave + 2 * tt + (p4 >> 1)) ^ swz(r0)) * 16) + 8 * (p4 & 1)); \
        const v2u hi = trread(L + L_BIG + r1 * 1024 + (((8 * wave + 2 * tt + (p4 >> 1)) ^ swz(r1)) * 16) + 8 * (p4 & 1)); \
        wf[t][tt] = v4u{lo.x, lo.y, hi.x, hi.y}; \
      } \
    } } while (0)
    if (wave != 0) { lds_wait(L, C_WO, 4); READ_WF(); }
    STAMP(7);
    {
      const int b = wave >> 1, chh = wave & 1, chunk = lane & 7, srow = lane >> 3;
      const u32 src = WS_IN3 + (h * 4 + j) * 32768 + (b * 32 + srow) * 256 + chh * 128 + chunk * 16;
      if (wave == 0) {
        v4u l0 = nv, l1 = nv;
        _Pragma("unroll 1") for (u32 spins = 0; spins < SPIN_CAP; ++spins) {
          CFENCE();
          if (BADTAG(l0) != 0u) l0 = __builtin_amdgcn_raw_buffer_load_b128(rs, WS_ST3 + h * 2048 + lane * 16, 0, 16);
          if (BADTAG(l1) != 0u) l1 = __builtin_amdgcn_raw_buffer_load_b128(rs, WS_ST3 + h * 2048 + 1024 + lane * 16, 0, 16);
          if (__all((BADTAG(l0) | BADTAG(l1)) == 0u)) break;
          __builtin_amdgcn_s_sleep(2);
        }
        *(LAS u32*)(L + M_LSE + lane * 4) = l0.x;
        *(LAS u32*)(L + M_LSE + 256 + lane * 4) = l1.x;
        lds_signal(L, C_LSE, lane);
      }
      lds_wait(L, C_LSE, 1);
      STAMP(10);
      v4u v[4]; u32 lb[4];
#pragma unroll
      for (int u = 0; u < 4; ++u) { v[u] = sv; lb[u] = *(const LAS u32*)(L + M_LSE + (b * 32 + 8 * u + srow) * 4); }
      _Pragma("unroll 1") for (u32 spins = 0; spins < SPIN_CAP; ++spins) {
        CFENCE();
#pragma unroll
        for (int u = 0; u < 4; ++u) if (umax4(v[u]) == SENT) v[u] = __builtin_amdgcn_raw_buffer_load_b128(rs, src + u * 2048, 0, 16);
        u32 mxv = 0u;
#pragma unroll
        for (int u = 0; u < 4; ++u) { const u32 t = umax4(v[u]); mxv = mxv > t ? mxv : t; }
        if (__all(mxv != SENT)) break;
        __builtin_amdgcn_s_sleep(2);
      }
      STAMP(8);
      float M = fmaxf(fmaxf(__uint_as_float(lb[0]), __uint_as_float(lb[1])), fmaxf(__uint_as_float(lb[2]), __uint_as_float(lb[3])));
      M = fmaxf(M, dpp<0x128>(M));
      M = maxx32(maxx16(M));
      float ex[4];
#pragma unroll
      for (int u = 0; u < 4; ++u) ex[u] = __builtin_amdgcn_exp2f(__uint_as_float(lb[u]) - M);
      float Ls = (ex[0] + ex[1]) + (ex[2] + ex[3]);
      Ls += dpp<0x128>(Ls);
      Ls = sumx32(sumx16(Ls));
      float oc[8];
#pragma unroll
      for (int e = 0; e < 8; ++e) oc[e] = 0.f;
#pragma unroll
      for (int u = 0; u < 4; ++u) {
        const float wv = ex[u];
        oc[0] += wv * lo_f(v[u].x); oc[1] += wv * hi_f(v[u].x); oc[2] += wv * lo_f(v[u].y); oc[3] += wv * hi_f(v[u].y);
        oc[4] += wv * lo_f(v[u].z); oc[5] += wv * hi_f(v[u].z); oc[6] += wv * lo_f(v[u].w); oc[7] += wv * hi_f(v[u].w);
      }
      float r4[4], r2[2];
#pragma unroll
      for (int e = 0; e < 4; ++e) r4[e] = tred32(oc[e], oc[4 + e]);
#pragma unroll
      for (int e = 0; e < 2; ++e) r2[e] = tred16(r4[e], r4[2 + e]);
      r2[0] += dpp<0x128>(r2[0]); r2[1] += dpp<0x128>(r2[1]);
      const float iL = __builtin_amdgcn_rcpf(Ls);
      if ((lane & 8) == 0) *(LAS u32*)(L + M_OCS + b * 256 + chh * 128 + chunk * 16 + gq * 4) = pk_bf16(r2[0] * iL, r2[1] * iL);
    }
    if (wave == 0) { lds_wait(L, C_WO, 4); READ_WF(); }
    BAR();
    {
      f4 c = {0.f, 0.f, 0.f, 0.f};
#pragma unroll
      for (int t = 0; t < 4; ++t) {
        const v4u of = *(const LAS v4u*)(L + M_OCS + (i16 & 3) * 256 + (32 * t + 8 * gq) * 2);
        c = mfma16(uvf[t], of, c);
      }
      if (i16 < 4) *(LAS v2u*)(L + M_CTX + (i16 * 128 + 16 * wave + 4 * gq) * 2) = v2u{pk_bf16(c[0], c[1]), pk_bf16(c[2], c[3])};
    }
    BAR();
    {
      f4 sacc[4];
#pragma unroll
      for (int tt = 0; tt < 4; ++tt) sacc[tt] = f4{0.f, 0.f, 0.f, 0.f};
#pragma unroll
      for (int t = 0; t < 4; ++t) {
        const v4u cf = *(const LAS v4u*)(L + M_CTX + ((i16 & 3) * 128 + 32 * t + 8 * gq) * 2);
#pragma unroll
        for (int tt = 0; tt < 4; ++tt) sacc[tt] = mfma16(wf[t][tt], cf, sacc[tt]);
      }
      LAS unsigned char* priv = L + L_P + wave * 1024;
      if (i16 < 4) {
#pragma unroll
        for (int tt = 0; tt < 4; ++tt) *(LAS f4*)(priv + ((tt * 4 + i16) * 4 + gq) * 16) = sacc[tt];
      }
      const v4u o = *(const LAS v4u*)(priv + lane * 16);
      const int f = ((lane >> 2) & 3) * 32 + 4 * wave + (lane >> 4);
      __builtin_amdgcn_raw_buffer_store_b128(nosent(o), rs, WS_IN5 + (f * 128 + wid) * 64 + (lane & 3) * 16, 0, 16);
    }
    STAMP(9);
  } else {
    const int kb = xg * 16 + idx, ab = kb >> 5, as = kb & 31;
    const bool do_ffn = idx < 14;
    const int fg = idx * 8 + xg;
    const u32 sz = swz(i16);
    v4u sf[16], vf[4][4];
    {
      const u16* kvp = ckv + ((size_t)ab * 4096 + as * 128 + wave * 4) * 512;
      const u32 ldst = lds_base + L_BIG + wave * 4096;
      const u32 rb = (u32)(wave & 3) * 4u;
      {
        const int ph = xg * 4 + (idx >> 2), pj = idx & 3, plw = wave >> 1, pr0 = 4 + 2 * (wave & 1);
        const u16* base = wq + (size_t)(256 * (pj >> 1) + 64 * plw) * 4096 + ph * 128 + 64 * (pj & 1);
#pragma unroll
        for (int r = 0; r < 2; ++r) glds16s(base, (u32)(8 * (pr0 + r) + (lane >> 3)) * 8192u + (u32)(lane & 7) * 16u, lds_base + L_DUMMY);
#pragma unroll
        for (int r = 0; r < 2; ++r) glds16s(wuk + (size_t)(128 * pj) * 4096 + ph * 128, (u32)(((plw * 8 + pr0 + r) * 4 + gq) * 8192 + i16 * 16), lds_base + L_DUMMY);
      }
      if (wave == 0 && lane < 32) __builtin_amdgcn_raw_buffer_store_b128(nv, rs, WS_ST3 + ((lane * 4 + ab) * 32 + as) * 16, 0, 16);
      __builtin_amdgcn_raw_buffer_store_b128(sv, rs, WS_IN5 + kb * 8192 + tid * 16, 0, 16);
#pragma unroll
      for (int t = 0; t < KDEPTH; ++t) {
#pragma unroll
        for (int r = 0; r < 4; ++r) glds16s(kvp + (size_t)(32 * t + r) * 512, (u32)((lane ^ swz(rb + r)) * 16), ldst + (32 * t + r) * 1024);
      }
      BAR();
#pragma unroll
      for (int t = 0; t < 4; ++t) {
        {
          const int younger = ((t + KDEPTH < 4 ? t + KDEPTH : 4) - 1 - t) * 4;
          if (younger == 12) VMWAIT(12); else if (younger == 8) VMWAIT(8); else if (younger == 4) VMWAIT(4); else VMWAIT(0);
        }
        lds_signal(L, C_KQ + t, lane);
        if (t == 3) {
#pragma unroll
          for (int e = 0; e < 4; ++e)
            __builtin_amdgcn_raw_buffer_store_b128(sv, rs, WS_IN3 + ((((8 * e + (lane >> 3)) * 4 + (wave >> 1)) * 4 + ab) * 32 + as) * 256 + (wave & 1) * 128 + (lane & 7) * 16, 0, 16);
        }
        if (t + KDEPTH < 4) {
#pragma unroll
          for (int r = 0; r < 4; ++r) glds16s(kvp + (size_t)(32 * (t + KDEPTH) + r) * 512, (u32)((lane ^ swz(rb + r)) * 16), ldst + (32 * (t + KDEPTH) + r) * 1024);
        }
        lds_wait(L, C_KQ + t, 8);
        if (t == 3) STAMP(2);
        const int k0 = 32 * t + 8 * gq + q4, k1 = k0 + 4;
#pragma unroll
        for (int tt = 0; tt < 4; ++tt) {
          const v2u lo = trread(L + L_BIG + k0 * 1024 + (((8 * wave + 2 * tt + (p4 >> 1)) ^ swz(k0)) * 16) + 8 * (p4 & 1));
          const v2u hi = trread(L + L_BIG + k1 * 1024 + (((8 * wave + 2 * tt + (p4 >> 1)) ^ swz(k1)) * 16) + 8 * (p4 & 1));
          vf[t][tt] = v4u{lo.x, lo.y, hi.x, hi.y};
        }
        if ((wave >> 1) == t) {
          LAS unsigned char* kvrow = L + L_BIG + (16 * wave + i16) * 1024;
#pragma unroll
          for (int u = 0; u < 16; ++u) sf[u] = *(const LAS v4u*)(kvrow + 256 * (u >> 2) + 16 * ((4 * (u & 3) + gq) ^ sz));
        }
        if (t == 0) lds_signal(L, C_PR, lane);
      }
    }
    STAMP(3);
    {
      const u32 src = WS_QA + (ab * 4096 + tid) * 16;
      v4u v[8];
#pragma unroll
      for (int u = 0; u < 8; ++u) v[u] = v4u{0u, tagA ^ 1u, 0u, 0u};
      _Pragma("unroll 1") for (u32 spins = 0; spins < SPIN_CAP; ++spins) {
        CFENCE();
        v[0] = __builtin_amdgcn_raw_buffer_load_b128(rs, src, 0, 16);
        if (__all(((v[0].y ^ tagA) | (v[0].w ^ tagB)) == 0u)) break;
        __builtin_amdgcn_s_sleep(2);
      }
      _Pragma("unroll 1") for (u32 spins = 0; spins < SPIN_CAP; ++spins) {
        CFENCE();
#pragma unroll
        for (int u = 1; u < 8; ++u)
          if (((v[u].y ^ tagA) | (v[u].w ^ tagB)) != 0u) v[u] = __builtin_amdgcn_raw_buffer_load_b128(rs, src, u * 8192, 16);
        u32 bad = 0u;
#pragma unroll
        for (int u = 0; u < 8; ++u) bad |= (v[u].y ^ tagA) | (v[u].w ^ tagB);
        if (__all(bad == 0u)) break;
        __builtin_amdgcn_s_sleep(1);
      }
      const int cu = tid & 127, hb = tid >> 7;
      lds_wait(L, C_PR, 8);
#pragma unroll
      for (int u = 0; u < 8; ++u) {
        const int hh = 4 * u + hb;
        *(LAS v2u*)(L + L_BIG + hh * 1024 + (((cu >> 1) ^ swz(hh)) * 16) + (cu & 1) * 8) = v2u{v[u].x, v[u].z};
      }
    }
    STAMP(4);
    VMWAIT(0);
    BAR();
    STAMP(5);
    f4 s0 = {0.f, 0.f, 0.f, 0.f}, s1 = s0;
    {
      LAS unsigned char* qrow = L + L_BIG + i16 * 1024;
      v4u qa[4], qb[4];
#pragma unroll
      for (int t = 0; t < 4; ++t) {
        const u32 off = 16 * ((4 * t + gq) ^ sz);
        qa[t] = *(const LAS v4u*)(qrow + off); qb[t] = *(const LAS v4u*)(qrow + 16384 + off);
      }
      __builtin_amdgcn_sched_barrier(0);
#pragma unroll
      for (int t = 0; t < 16; ++t) {
        s0 = mfma16(sf[t], qa[t & 3], s0);
        s1 = mfma16(sf[t], qb[t & 3], s1);
        if (t + 4 < 16) {
          const u32 off = 256 * ((t + 4) >> 2) + 16 * ((4 * (t & 3) + gq) ^ sz);
          qa[t & 3] = *(const LAS v4u*)(qrow + off); qb[t & 3] = *(const LAS v4u*)(qrow + 16384 + off);
        }
        __builtin_amdgcn_sched_barrier(0);
      }
    }
    {
      float mx0 = maxx32(maxx16(fmaxf(fmaxf(s0[0], s0[1]), fmaxf(s0[2], s0[3]))));
      float mx1 = maxx32(maxx16(fmaxf(fmaxf(s1[0], s1[1]), fmaxf(s1[2], s1[3]))));
      if (lane < 32) *(LAS float*)(L + M_WMAX + (lane * 8 + wave) * 4) = lane < 16 ? mx0 : mx1;
    }
    STAMP(6);
    v4u fu = {0u, 0u, 0u, 0u}, wd0 = fu, wd1 = fu, xv = fu, wrv = fu;
    if (do_ffn) {
      fu = __builtin_amdgcn_raw_buffer_load_b128(rs, WS_FX + fg * 8192 + tid * 16, 0, 16);
      wd0 = ldnt(wd + (size_t)(16 * fg + 2 * (lane >> 3)) * 512 + 64 * wave + 8 * (lane & 7));
      wd1 = ldnt(wd + (size_t)(16 * fg + 2 * (lane >> 3) + 1) * 512 + 64 * wave + 8 * (lane & 7));
    }
    const u16 xfin = x[ab * 512 + as * 16 + (tid & 15)];
    if (wave < 4) {
      xv = *(const GAS v4u*)(x + wave * 512 + lane * 8);
      wrv = *(const GAS v4u*)(wr + lane * 8);
    }
    __builtin_amdgcn_sched_barrier(0);
    BAR();
    float m0, m1;
    {
      const f4 a0 = *(const LAS f4*)(L + M_WMAX + i16 * 32), a1 = *(const LAS f4*)(L + M_WMAX + i16 * 32 + 16);
      const f4 b0 = *(const LAS f4*)(L + M_WMAX + 512 + i16 * 32), b1 = *(const LAS f4*)(L + M_WMAX + 512 + i16 * 32 + 16);
      m0 = fmaxf(fmaxf(fmaxf(a0[0], a0[1]), fmaxf(a0[2], a0[3])), fmaxf(fmaxf(a1[0], a1[1]), fmaxf(a1[2], a1[3])));
      m1 = fmaxf(fmaxf(fmaxf(b0[0], b0[1]), fmaxf(b0[2], b0[3])), fmaxf(fmaxf(b1[0], b1[1]), fmaxf(b1[2], b1[3])));
      const u32 pa = pk_bf16(__builtin_amdgcn_exp2f(s0[0] - m0), __builtin_amdgcn_exp2f(s0[1] - m0));
      const u32 pb = pk_bf16(__builtin_amdgcn_exp2f(s0[2] - m0), __builtin_amdgcn_exp2f(s0[3] - m0));
      const u32 pc = pk_bf16(__builtin_amdgcn_exp2f(s1[0] - m1), __builtin_amdgcn_exp2f(s1[1] - m1));
      const u32 pd = pk_bf16(__builtin_amdgcn_exp2f(s1[2] - m1), __builtin_amdgcn_exp2f(s1[3] - m1));
      const u32 poff = i16 * 256 + (((2 * wave + (gq >> 1)) ^ sz) * 16) + (gq & 1) * 8;
      *(LAS v2u*)(L + L_P + poff) = v2u{pa, pb};
      *(LAS v2u*)(L + L_P + 4096 + poff) = v2u{pc, pd};
      const float l0 = sumx32(sumx16((lo_f(pa) + hi_f(pa)) + (lo_f(pb) + hi_f(pb))));
      const float l1 = sumx32(sumx16((lo_f(pc) + hi_f(pc)) + (lo_f(pd) + hi_f(pd))));
      if (lane < 32) *(LAS float*)(L + M_WSUM + (lane * 8 + wave) * 4) = lane < 16 ? l0 : l1;
    }
    BAR();
    STAMP(7);
    if (wave == 0 && lane < 32) {
      const f4 la = *(const LAS f4*)(L + M_WSUM + lane * 32), lb = *(const LAS f4*)(L + M_WSUM + lane * 32 + 16);
      const float l = ((la[0] + la[1]) + (la[2] + la[3])) + ((lb[0] + lb[1]) + (lb[2] + lb[3]));
      const u32 lse = __float_as_uint((lane < 16 ? m0 : m1) + __builtin_amdgcn_logf(l));
      __builtin_amdgcn_raw_buffer_store_b128(v4u{lse, tagA, lse, tagB}, rs, WS_ST3 + ((lane * 4 + ab) * 32 + as) * 16, 0, 16);
    }
#pragma unroll
    for (int ht = 0; ht < 2; ++ht) {
      f4 oacc[4];
#pragma unroll
      for (int tt = 0; tt < 4; ++tt) oacc[tt] = f4{0.f, 0.f, 0.f, 0.f};
#pragma unroll
      for (int t = 0; t < 4; ++t) {
        const v4u pf = *(const LAS v4u*)(L + L_P + ht * 4096 + i16 * 256 + (((4 * t + gq) ^ sz) * 16));
#pragma unroll
        for (int tt = 0; tt < 4; ++tt) oacc[tt] = mfma16(vf[t][tt], pf, oacc[tt]);
      }
      const f4 la = *(const LAS f4*)(L + M_WSUM + ht * 512 + i16 * 32), lb = *(const LAS f4*)(L + M_WSUM + ht * 512 + i16 * 32 + 16);
      const float l = ((la[0] + la[1]) + (la[2] + la[3])) + ((lb[0] + lb[1]) + (lb[2] + lb[3]));
      const float il = __builtin_amdgcn_rcpf(l);
      const int head = 16 * ht + i16;
      LAS unsigned char* priv = L + L_BIG + 65536 + wave * 4608;
#pragma unroll
      for (int tt = 0; tt < 4; ++tt)
        *(LAS v2u*)(priv + head * 144 + tt * 32 + gq * 8) = v2u{pk_bf16(oacc[tt][0] * il, oacc[tt][1] * il), pk_bf16(oacc[tt][2] * il, oacc[tt][3] * il)};
#pragma unroll
      for (int e = 0; e < 2; ++e) {
        const int hd = 16 * ht + 8 * e + (lane >> 3);
        const v4u o = *(const LAS v4u*)(priv + hd * 144 + (lane & 7) * 16);
        __builtin_amdgcn_raw_buffer_store_b128(nosent(o), rs,
            WS_IN3 + (((hd * 4 + (wave >> 1)) * 4 + ab) * 32 + as) * 256 + (wave & 1) * 128 + (lane & 7) * 16, 0, 16);
      }
    }
    STAMP(8);
    if (wave < 4) ln_row(L, xv, wrv, wave, lane);
    if (do_ffn) {
      const int chunk = lane & 7, rg = lane >> 3;
      _Pragma("unroll 1") for (u32 spins = 0; spins < SPIN_CAP; ++spins) {
        if (__all(((fu.y ^ tagA) | (fu.w ^ tagB)) == 0u)) break;
        CFENCE();
        fu = __builtin_amdgcn_raw_buffer_load_b128(rs, WS_FX + fg * 8192 + tid * 16, 0, 16);
      }
      *(LAS v2u*)(L + L_BIG + 32768 + tid * 8) = v2u{fu.x, fu.z};
      BAR();
      if (lane < 32) {
        float g0 = 0.f, g1 = 0.f, u0 = 0.f, u1 = 0.f;
#pragma unroll
        for (int w = 0; w < 8; ++w) {
          const v2u gv = *(const LAS v2u*)(L + L_BIG + 32768 + (w * 64 + lane) * 8), uv = *(const LAS v2u*)(L + L_BIG + 32768 + (w * 64 + 32 + lane) * 8);
          g0 += __uint_as_float(gv.x); g1 += __uint_as_float(gv.y); u0 += __uint_as_float(uv.x); u1 += __uint_as_float(uv.y);
        }
        g0 = rbf(g0); g1 = rbf(g1);
        const float h0 = rbf(rbf(g0 / (1.0f + __expf(-g0))) * rbf(u0)), h1 = rbf(rbf(g1 / (1.0f + __expf(-g1))) * rbf(u1));
        *(LAS v2u*)(L + L_BIG + 40960 + wave * 256 + lane * 8) = v2u{__float_as_uint(h0), __float_as_uint(h1)};
      }
      float y[32];
#pragma unroll
      for (int b = 0; b < 4; ++b) {
        const v2u hp = *(const LAS v2u*)(L + L_BIG + 40960 + wave * 256 + (b * 16 + 2 * rg) * 4);
        const float h0 = __uint_as_float(hp.x), h1 = __uint_as_float(hp.y);
#pragma unroll
        for (int e = 0; e < 4; ++e) {
          y[b * 8 + 2 * e] = h0 * lo_f(wd0[e]) + h1 * lo_f(wd1[e]);
          y[b * 8 + 2 * e + 1] = h0 * hi_f(wd0[e]) + h1 * hi_f(wd1[e]);
        }
      }
      float n16[16], n8[8];
#pragma unroll
      for (int k = 0; k < 16; ++k) n16[k] = tred32(y[k], y[16 + k]);
#pragma unroll
      for (int k = 0; k < 8; ++k) { n8[k] = tred16(n16[k], n16[8 + k]); n8[k] += dpp<0x128>(n8[k]); }
      LAS unsigned char* priv = L + L_BIG + 65536 + wave * 4608;
      const f4 oh = (lane & 8) ? f4{n8[4], n8[5], n8[6], n8[7]} : f4{n8[0], n8[1], n8[2], n8[3]};
      *(LAS f4*)(priv + (gq * 16 + chunk * 2 + ((lane >> 3) & 1)) * 16) = oh;
      const v4u o = *(const LAS v4u*)(priv + lane * 16);
      const int f = gq * 32 + 4 * wave + (i16 >> 2);
      __builtin_amdgcn_raw_buffer_store_b128(nosent(o), rs, WS_INY + (f * 112 + fg) * 64 + (i16 & 3) * 16, 0, 16);
    }
    STAMP(9);
    float gate = 0.f;
    {
      const u32 sy = WS_INY + kb * 7168 + (tid < 448 ? tid : 0) * 16;
      v4u y0 = sv;
      __builtin_amdgcn_s_sleep(8);
      _Pragma("unroll 1") for (u32 spins = 0; spins < SPIN_CAP; ++spins) {
        CFENCE();
        if (umax4(y0) == SENT) y0 = __builtin_amdgcn_raw_buffer_load_b128(rs, sy, 0, 16);
        if (__all(umax4(y0) != SENT)) break;
        __builtin_amdgcn_s_sleep(4);
      }
      float v[4], n2[2];
#pragma unroll
      for (int e = 0; e < 4; ++e) v[e] = tid < 448 ? __uint_as_float(y0[e]) : 0.f;
#pragma unroll
      for (int e = 0; e < 2; ++e) n2[e] = tred32(v[e], v[2 + e]);
      float n1 = tred16(n2[0], n2[1]);
      n1 += dpp<0x124>(n1); n1 += dpp<0x128>(n1);
      if (i16 < 4) *(LAS float*)(L + M_FIN + (wave * 16 + i16 * 4 + gq) * 4) = n1;
      BAR();
      if (tid < 16) {
        const int m = as * 16 + tid;
        float y = 0.f;
#pragma unroll
        for (int w = 0; w < 8; ++w) y += *(const LAS float*)(L + M_FIN + (w * 16 + tid) * 4);
        const float rsv = *(const LAS float*)(L + M_RS + ab * 4);
        const float xnv = bf2f(*(const LAS u16*)(L + L_XN + (ab * 512 + m) * 2));
        y = rbf(rbf(y) * rsv);
        const float z = rbf(xnv + y);
        gate = rbf(z / (1.0f + __expf(-z)));
      }
    }
    STAMP(13);
    {
      const u32 s5 = WS_IN5 + kb * 8192 + tid * 16;
      v4u a0 = sv;
      _Pragma("unroll 1") for (u32 spins = 0; spins < SPIN_CAP; ++spins) {
        CFENCE();
        if (umax4(a0) == SENT) a0 = __builtin_amdgcn_raw_buffer_load_b128(rs, s5, 0, 16);
        if (__all(umax4(a0) != SENT)) break;
        __builtin_amdgcn_s_sleep(1);
      }
      STAMP(10);
      float n2[2];
#pragma unroll
      for (int e = 0; e < 2; ++e) n2[e] = tred32(__uint_as_float(a0[e]), __uint_as_float(a0[2 + e]));
      float n1 = tred16(n2[0], n2[1]);
      n1 += dpp<0x124>(n1); n1 += dpp<0x128>(n1);
      if (i16 < 4) *(LAS float*)(L + M_FIN + 512 + (wave * 16 + i16 * 4 + gq) * 4) = n1;
    }
    BAR();
    if (tid < 16) {
      float sacc = 0.f;
#pragma unroll
      for (int w = 0; w < 8; ++w) sacc += *(const LAS float*)(L + M_FIN + 512 + (w * 16 + tid) * 4);
      const float o = bf2f(xfin) + rbf(rbf(sacc) * gate);
      out[ab * 512 + as * 16 + tid] = __builtin_bit_cast(u16, (__bf16)o);
    }
    if (g == 0 && tid == 0) __builtin_amdgcn_raw_buffer_store_b64(v2u{tagA, tagB}, rs, WS_EPOCH, 0, 16);
  }
}

extern "C" void kernel_launch(void* const* d_in, const int* in_sizes, int n_in,
                              void* d_out, int out_size, void* d_ws, size_t ws_size,
                              hipStream_t stream) {
  (void)in_sizes; (void)n_in; (void)out_size;
  (void)hipFuncSetAttribute((const void*)mega, hipFuncAttributeMaxDynamicSharedMemorySize, LDS_BYTES);
  u64* st = (u64*)((char*)d_ws + ws_size - (1 << 20));
  mega<<<NWG, 512, LDS_BYTES, stream>>>((const u16*)d_in[0], (const u16*)d_in[1], (const u16*)d_in[2], (const u16*)d_in[3],
                                        (const u16*)d_in[4], (const u16*)d_in[5], (const u16*)d_in[7], (const u16*)d_in[8],
                                        (const u16*)d_in[9], (const u16*)d_in[10], (u16*)d_out, (unsigned char*)d_ws, st);
}
```

```cpp
#include <hip/hip_runtime.h>
#include <stdint.h>

typedef unsigned short u16;
typedef unsigned int u32;
typedef unsigned long long u64;
typedef u32 v4u __attribute__((ext_vector_type(4)));
typedef u32 v2u __attribute__((ext_vector_type(2)));
typedef float f4 __attribute__((ext_vector_type(4)));
typedef short bf16x8 __attribute__((ext_vector_type(8)));
typedef short s16x4 __attribute__((ext_vector_type(4)));
typedef __bf16 bf16x2 __attribute__((ext_vector_type(2)));
#define LAS __attribute__((address_space(3)))
#define GAS __attribute__((address_space(1)))

#define STAMPS 0

#define WODELAY 0
#define CLRPOS 3
#define KDEPTH 3
#define CPF 1
#define CPF_KB 22
#define KVDELAY 100
constexpr int NWG = 256;
constexpr u32 WS_EPOCH = 0;
constexpr u32 WS_CF = 2048;
constexpr u32 WS_QX = 4096;
constexpr u32 WS_FX = WS_QX + 32 * 8192;
constexpr u32 WS_QA = WS_FX + 112 * 8192;
constexpr u32 WS_IN3 = WS_QA + 4 * 32 * 128 * 16;
constexpr u32 WS_ST3 = WS_IN3 + 32 * 4 * 32768;
constexpr u32 WS_IN5 = WS_ST3 + 32 * 2048;
constexpr u32 WS_INY = WS_IN5 + 128 * 8192;
constexpr u32 WS_END = WS_INY + 128 * 7168;
constexpr int L_BIG = 0;
constexpr int L_XN = 131072;
constexpr int L_P = L_XN + 4096;
constexpr int L_M = L_P + 8192;
constexpr int M_RED = L_P;
constexpr int M_QS = L_M;
constexpr int M_LSE = L_M + 1024;
constexpr int M_WMAX = L_M + 2048;
constexpr int M_WSUM = L_M + 3072;
constexpr int M_OCS = L_M + 4096;
constexpr int M_CTX = L_M + 5120;
constexpr int M_H = L_M + 6144;
constexpr int M_FIN = L_M + 6400;
constexpr int M_RS = L_M + 7424;
constexpr int M_CNT = L_M + 7488;
constexpr int L_DUMMY = L_M + 7680;
constexpr int LDS_BYTES = L_DUMMY + 1024;
enum { C_XN = 0, C_T = 1, C_RED = 2, C_CLR = 3, C_KV = 4, C_A = 5, C_UV = 6, C_UVR = 7, C_F = 8, C_FR = 9, C_H = 10, C_T2 = 11, C_QS = 12, C_LSE = 13, C_FA = 14, C_PR = 15, C_KQ = 16  , C_WO = 20 };
constexpr u32 SENT = 0xFFFFFFFFu;
constexpr u32 SPIN_CAP = 1u << 16;

__device__ __forceinline__ float bf2f(u32 v) { return __uint_as_float(v << 16); }
__device__ __forceinline__ float lo_f(u32 v) { return __uint_as_float(v << 16); }
__device__ __forceinline__ float hi_f(u32 v) { return __uint_as_float(v & 0xffff0000u); }
__device__ __forceinline__ u32 pk_bf16(float a, float b) {
  bf16x2 r; r[0] = (__bf16)a; r[1] = (__bf16)b; return __builtin_bit_cast(u32, r);
}
__device__ __forceinline__ float rbf(float a) { return (float)((__bf16)a); }
__device__ __forceinline__ f4 mfma16(v4u a, v4u b, f4 c) {
  return __builtin_amdgcn_mfma_f32_16x16x32_bf16(__builtin_bit_cast(bf16x8, a), __builtin_bit_cast(bf16x8, b), c, 0, 0, 0);
}
__device__ __forceinline__ float dot2(u32 a, u32 b, float c) {
  return __builtin_amdgcn_fdot2_f32_bf16(__builtin_bit_cast(bf16x2, a), __builtin_bit_cast(bf16x2, b), c, false);
}
__device__ __forceinline__ u32 swz(u32 row) { return ((((row >> 3) ^ (row >> 2)) & 1u) << 3) | ((row & 3u) << 1) | ((row >> 3) & 1u); }
__device__ __forceinline__ v2u trread(LAS unsigned char* p) {
  return __builtin_bit_cast(v2u, __builtin_amdgcn_ds_read_tr16_b64_v4i16((LAS s16x4*)p));
}
__device__ __forceinline__ void glds16s(const void* sbase, u32 voff, u32 lds_dst) {
  u32 keep;
  asm volatile("s_mov_b32 %0, m0\n\ts_mov_b32 m0, %3\n\ts_nop 0\n\tglobal_load_lds_dwordx4 %1, %2 nt\n\ts_mov_b32 m0, %0"
               : "=&s"(keep) : "v"(voff), "s"(sbase), "s"(lds_dst) : "memory");
}
__device__ __forceinline__ void glds16v(const void* gsrc, u32 lds_dst) {
  u32 keep;
  asm volatile("s_mov_b32 %0, m0\n\ts_mov_b32 m0, %2\n\ts_nop 0\n\tglobal_load_lds_dwordx4 %1, off nt\n\ts_mov_b32 m0, %0"
               : "=&s"(keep) : "v"(gsrc), "s"(lds_dst) : "memory");
}
__device__ __forceinline__ v4u ldnt(const void* p) { return __builtin_nontemporal_load((const GAS v4u*)p); }
template <int CTRL> __device__ __forceinline__ float dpp(float v) {
  return __int_as_float(__builtin_amdgcn_update_dpp(0, __float_as_int(v), CTRL, 0xf, 0xf, true));
}
__device__ __forceinline__ float sum16(float v) { v += dpp<0xB1>(v); v += dpp<0x4E>(v); v += dpp<0x141>(v); v += dpp<0x140>(v); return v; }
__device__ __forceinline__ float sumx16(float v) { auto r = __builtin_amdgcn_permlane16_swap(__float_as_uint(v), __float_as_uint(v), false, false); return __uint_as_float(r[0]) + __uint_as_float(r[1]); }
__device__ __forceinline__ float sumx32(float v) { auto r = __builtin_amdgcn_permlane32_swap(__float_as_uint(v), __float_as_uint(v), false, false); return __uint_as_float(r[0]) + __uint_as_float(r[1]); }
__device__ __forceinline__ float maxx16(float v) { auto r = __builtin_amdgcn_permlane16_swap(__float_as_uint(v), __float_as_uint(v), false, false); return fmaxf(__uint_as_float(r[0]), __uint_as_float(r[1])); }
__device__ __forceinline__ float maxx32(float v) { auto r = __builtin_amdgcn_permlane32_swap(__float_as_uint(v), __float_as_uint(v), false, false); return fmaxf(__uint_as_float(r[0]), __uint_as_float(r[1])); }
__device__ __forceinline__ float sum64(float v) { return sumx32(sumx16(sum16(v))); }
__device__ __forceinline__ float tred32(float a, float b) { auto r = __builtin_amdgcn_permlane32_swap(__float_as_uint(a), __float_as_uint(b), false, false); return __uint_as_float(r[0]) + __uint_as_float(r[1]); }
__device__ __forceinline__ float tred16(float a, float b) { auto r = __builtin_amdgcn_permlane16_swap(__float_as_uint(a), __float_as_uint(b), false, false); return __uint_as_float(r[0]) + __uint_as_float(r[1]); }

#define BAR() do { asm volatile("s_waitcnt lgkmcnt(0)" ::: "memory"); __builtin_amdgcn_s_barrier(); asm volatile("" ::: "memory"); } while (0)
#define BADTAG(v) (((v).y ^ tagA) | ((v).w ^ tagB))
#define CFENCE() asm volatile("" ::: "memory")
#define VMWAIT(n) asm volatile("s_waitcnt vmcnt(" #n ")" ::: "memory")
__device__ __forceinline__ u32 umax4(v4u v) { u32 a = v.x > v.y ? v.x : v.y, b = v.z > v.w ? v.z : v.w; return a > b ? a : b; }
__device__ __forceinline__ v4u nosent(v4u v) {
  v.x = v.x < 0xFFFFFFFEu ? v.x : 0xFFFFFFFEu; v.y = v.y < 0xFFFFFFFEu ? v.y : 0xFFFFFFFEu;
  v.z = v.z < 0xFFFFFFFEu ? v.z : 0xFFFFFFFEu; v.w = v.w < 0xFFFFFFFEu ? v.w : 0xFFFFFFFEu; return v;
}
__device__ __forceinline__ void lds_signal(LAS unsigned char* L, int c, int lane) {
  asm volatile("s_waitcnt lgkmcnt(0)" ::: "memory");
  if (lane == 0) __hip_atomic_fetch_add((LAS u32*)(L + M_CNT) + c, 1u, __ATOMIC_RELAXED, __HIP_MEMORY_SCOPE_WORKGROUP);
}
__device__ __forceinline__ void lds_wait(LAS unsigned char* L, int c, u32 target) {
  _Pragma("unroll 1") for (u32 spins = 0; spins < (1u << 24); ++spins) {
    if (*((volatile LAS u32*)(L + M_CNT) + c) >= target) break;
    __builtin_amdgcn_s_sleep(0);
  }
  CFENCE();
}

__device__ __forceinline__ void ln_row(LAS unsigned char* L, v4u xv, v4u wrv, int r, int lane) {
  float xf[8] = {lo_f(xv.x), hi_f(xv.x), lo_f(xv.y), hi_f(xv.y), lo_f(xv.z), hi_f(xv.z), lo_f(xv.w), hi_f(xv.w)};
  float s = ((xf[0] + xf[1]) + (xf[2] + xf[3])) + ((xf[4] + xf[5]) + (xf[6] + xf[7]));
  const float mean = sum64(s) * (1.0f / 512.0f);
  float q2 = 0.f;
#pragma unroll
  for (int e = 0; e < 8; ++e) { const float d = xf[e] - mean; q2 += d * d; }
  const float var = rbf(sum64(q2) * (1.0f / 512.0f));
  const float mb = rbf(mean);
  const float den = rbf(sqrtf(rbf(var + 1e-5f)));
  float xn[8];
#pragma unroll
  for (int e = 0; e < 8; ++e) xn[e] = rbf(xf[e] - mb) / den;
  v4u xnp = {pk_bf16(xn[0], xn[1]), pk_bf16(xn[2], xn[3]), pk_bf16(xn[4], xn[5]), pk_bf16(xn[6], xn[7])};
  *(LAS v4u*)(L + L_XN + r * 1024 + lane * 16) = xnp;
  lds_signal(L, C_XN, lane);
  float rr = dot2(xnp.x, wrv.x, 0.f); rr = dot2(xnp.y, wrv.y, rr); rr = dot2(xnp.z, wrv.z, rr); rr = dot2(xnp.w, wrv.w, rr);
  rr = rbf(sum64(rr));
  if (lane == 0) *(LAS float*)(L + M_RS + r * 4) = rbf(1.0f / (1.0f + __expf(-rr)));
}

__device__ __forceinline__ u32 gsw(u32 row) { return (((row >> 1) & 1u) | (((row >> 3) & 1u) << 1)) << 1; }
__device__ __forceinline__ void tile_gemv(LAS unsigned char* L, int tile_off, int m0, int kq, int lane) {
  const int i16 = lane & 15, gq = lane >> 4, q4 = i16 >> 2, p4 = i16 & 3;
  f4 acc[4];
#pragma unroll
  for (int dt = 0; dt < 4; ++dt) acc[dt] = f4{0.f, 0.f, 0.f, 0.f};
  const u32 fl = gsw(8 * gq + q4);
#pragma unroll
  for (int t = 0; t < 2; ++t) {
    const int r0 = 64 * kq + 32 * t + 8 * gq + q4;
    const v4u xf = *(const LAS v4u*)(L + L_XN + (i16 & 3) * 1024 + (m0 + 64 * kq + 32 * t + 8 * gq) * 2);
    LAS unsigned char* b0 = L + tile_off + r0 * 128 + 8 * (p4 & 1);
#pragma unroll
    for (int dt = 0; dt < 4; ++dt) {
      const v2u a0 = trread(b0 + (((2 * dt + (p4 >> 1)) ^ fl) * 16)), a1 = trread(b0 + 512 + (((2 * dt + (p4 >> 1)) ^ fl) * 16));
      acc[dt] = mfma16(v4u{a0.x, a0.y, a1.x, a1.y}, xf, acc[dt]);
    }
  }
  if (i16 < 4) {
#pragma unroll
    for (int dt = 0; dt < 4; ++dt) *(LAS f4*)(L + M_RED + (((kq * 4 + dt) * 4 + gq) * 4 + i16) * 16) = acc[dt];
  }
}

__global__ __launch_bounds__(512, 2) void mega(const u16* __restrict__ x, const u16* __restrict__ ckv, const u16* __restrict__ wq,
                                               const u16* __restrict__ wuk, const u16* __restrict__ wuv, const u16* __restrict__ wo,
                                               const u16* __restrict__ wr, const u16* __restrict__ wg, const u16* __restrict__ wu,
                                               const u16* __restrict__ wd, u16* __restrict__ out, unsigned char* ws, u64* stamps) {
  extern __shared__ __attribute__((aligned(16))) unsigned char lds_raw[];
  LAS unsigned char* L = (LAS unsigned char*)lds_raw;
  const int tid = threadIdx.x, lane = tid & 63;
  const int wave = __builtin_amdgcn_readfirstlane(tid >> 6);
  const int g = blockIdx.x, xg = g & 7, idx = g >> 3;
  const int i16 = lane & 15, gq = lane >> 4, q4 = i16 >> 2, p4 = i16 & 3;
  const __amdgpu_buffer_rsrc_t rs = __builtin_amdgcn_make_buffer_rsrc((void*)ws, 0, (int)WS_END, 0x00020000);
  const u32 lds_base = (u32)(size_t)lds_raw;
  const v4u sv = {SENT, SENT, SENT, SENT};
#define STAMP(k)
  {
    u64 pc;
    asm volatile("s_getpc_b64 %0" : "=s"(pc));
    const int slice = idx + 32 * (wave >> 2);
    if ((wave & 3) == 3 && slice < CPF_KB) {
      u32 keep;
      asm volatile("s_mov_b32 %0, m0\n\ts_mov_b32 m0, %3\n\ts_nop 0\n\tglobal_load_lds_dwordx4 %1, %2\n\ts_mov_b32 m0, %0"
                   : "=&s"(keep) : "v"((u32)(slice * 1024 + lane * 16)), "s"(pc & ~(u64)63), "s"(lds_base + L_DUMMY) : "memory");
    }
  }
  STAMP(0);
  if (tid < 24) *((LAS u32*)(L + M_CNT) + tid) = 0u;
  const u64 tk0 = __builtin_amdgcn_s_memrealtime();
  const v2u ep = *(const __attribute__((address_space(4))) v2u*)(ws + WS_EPOCH);

  const u32 tagA = ep.x + 1u, tagB = ep.y + 1u;
  const v4u nv = {0u, tagA ^ 1u, 0u, 0u};
  if (idx >= 16) {
    const int wi = idx - 16, h = xg * 4 + (wi >> 2), j = wi & 3, wid = xg * 16 + wi;
    if (wave < 4) {
      const int a = wave;
      const v4u xv = *(const GAS v4u*)(x + a * 512 + lane * 8);
      const v4u wrv = *(const GAS v4u*)(wr + lane * 8);
      BAR();
      ln_row(L, xv, wrv, a, lane);
      lds_wait(L, C_XN, 4);
      lds_wait(L, C_T, 4);
      STAMP(1);
      tile_gemv(L, L_BIG, 256 * (j >> 1), a, lane);
      lds_signal(L, C_RED, lane);
      lds_wait(L, C_RED, 4);
      if (lane < 32) {
        float s0 = 0.f, s1 = 0.f;
#pragma unroll
        for (int kq = 0; kq < 4; ++kq) {
          const v2u pv = *(const LAS v2u*)(L + M_RED + ((kq * 16 + (lane >> 1)) * 4 + a) * 16 + (lane & 1) * 8);
          s0 += __uint_as_float(pv.x); s1 += __uint_as_float(pv.y);
        }
        __builtin_amdgcn_raw_buffer_store_b128(v4u{__float_as_uint(s0), tagA, __float_as_uint(s1), tagB}, rs,
                                               WS_QX + (((h * 4 + a) * 2 + (j >> 1)) * 64 + 32 * (j & 1) + lane) * 16, 0, 16);
      }
      STAMP(2);
      lds_wait(L, C_T2, 4);
      v4u wukf[2][4];
#pragma unroll
      for (int ct = 0; ct < 2; ++ct)
#pragma unroll
        for (int t = 0; t < 4; ++t)
          wukf[ct][t] = *(const LAS v4u*)(L + L_BIG + 32768 + (32 * a + 16 * ct + i16) * 256 + (((4 * t + gq) ^ swz(i16)) * 16));
      v4u qu[2];
      qu[0] = qu[1] = v4u{0u, tagA ^ 1u, 0u, 0u};
      const u32 qsrc = WS_QX + ((h * 4 + a) * 128 + lane) * 16;
      _Pragma("unroll 1") for (u32 spins = 0; spins < SPIN_CAP; ++spins) {
        CFENCE();
#pragma unroll
        for (int u = 0; u < 2; ++u)
          if (((qu[u].y ^ tagA) | (qu[u].w ^ tagB)) != 0u) qu[u] = __builtin_amdgcn_raw_buffer_load_b128(rs, qsrc + u * 1024, 0, 16);
        const u32 bad = (qu[0].y ^ tagA) | (qu[0].w ^ tagB) | (qu[1].y ^ tagA) | (qu[1].w ^ tagB);
        if (__all(bad == 0u)) break;
      }
      *(LAS u32*)(L + M_QS + a * 256 + lane * 4) = pk_bf16(__uint_as_float(qu[0].x) + __uint_as_float(qu[1].x), __uint_as_float(qu[0].z) + __uint_as_float(qu[1].z));
      lds_signal(L, C_QS, lane);
      lds_wait(L, C_QS, 4);
      STAMP(3);
      LAS unsigned char* priv = L + L_BIG + 65536 + a * 2048;
      f4 qa0 = {0.f, 0.f, 0.f, 0.f}, qa1 = qa0;
#pragma unroll
      for (int t = 0; t < 4; ++t) {
        const v4u qf = *(const LAS v4u*)(L + M_QS + (i16 & 3) * 256 + (32 * t + 8 * gq) * 2);
        qa0 = mfma16(wukf[0][t], qf, qa0);
        qa1 = mfma16(wukf[1][t], qf, qa1);
      }
      const float SC = 0.08838834764831845f * 1.4426950408889634f;
      if (i16 < 4) {
        *(LAS v2u*)(priv + 1024 + (i16 * 8 + gq) * 8) = v2u{pk_bf16(qa0[0] * SC, qa0[1] * SC), pk_bf16(qa0[2] * SC, qa0[3] * SC)};
        *(LAS v2u*)(priv + 1024 + (i16 * 8 + 4 + gq) * 8) = v2u{pk_bf16(qa1[0] * SC, qa1[1] * SC), pk_bf16(qa1[2] * SC, qa1[3] * SC)};
      }
      lds_wait(L, C_CLR, 4);
      if (lane < 32) {
        const v2u pv = *(const LAS v2u*)(priv + 1024 + lane * 8);
        __builtin_amdgcn_raw_buffer_store_b128(v4u{pv.x, tagA, pv.y, tagB}, rs, WS_QA + (((lane >> 3) * 32 + h) * 128 + 32 * j + 8 * a + (lane & 7)) * 16, 0, 16);
      }
      lds_signal(L, C_A, lane);
      STAMP(4);
      if (wi < 14) {
        while ((long long)(__builtin_amdgcn_s_memrealtime() - tk0) < WODELAY) __builtin_amdgcn_s_sleep(2);
        v4u fw[8];
        const int mat = wave & 1, ks = wave >> 1;
        const u16* src = (mat ? wu : wg) + (size_t)(64 * xg + 32 * ks + 8 * gq) * 1792 + 128 * wi + 8 * i16;
#pragma unroll
        for (int jj = 0; jj < 8; ++jj) fw[jj] = ldnt(src + (size_t)jj * 1792);
        const v4u af = *(const LAS v4u*)(L + L_XN + (i16 >> 2) * 1024 + (64 * xg + 32 * ks + 8 * gq) * 2);
        float pr[8];
#pragma unroll
        for (int cc = 0; cc < 8; ++cc) {
          const u32 sel = (cc & 1) ? 0x07060302u : 0x05040100u;
          v4u bfr;
          bfr.x = __builtin_amdgcn_perm(fw[1][cc >> 1], fw[0][cc >> 1], sel);
          bfr.y = __builtin_amdgcn_perm(fw[3][cc >> 1], fw[2][cc >> 1], sel);
          bfr.z = __builtin_amdgcn_perm(fw[5][cc >> 1], fw[4][cc >> 1], sel);
          bfr.w = __builtin_amdgcn_perm(fw[7][cc >> 1], fw[6][cc >> 1], sel);
          pr[cc] = mfma16(af, bfr, f4{0.f, 0.f, 0.f, 0.f})[0];
        }
        LAS f4* dst = (LAS f4*)(L + M_RED + ((wave * 4 + gq) * 128 + 8 * i16) * 4);
        dst[0] = f4{pr[0], pr[1], pr[2], pr[3]};
        dst[1] = f4{pr[4], pr[5], pr[6], pr[7]};
        lds_signal(L, C_FA, lane);
        lds_wait(L, C_FA, 4);
#pragma unroll
        for (int u = 0; u < 2; ++u) {
          const int U = ks * 128 + u * 64 + lane, msd = U >> 5, b = (U >> 3) & 3, k = U & 7;
          const v2u p0 = *(const LAS v2u*)(L + M_RED + (((0 + mat) * 4 + b) * 128 + 16 * msd + 2 * k) * 4);
          const v2u p1 = *(const LAS v2u*)(L + M_RED + (((2 + mat) * 4 + b) * 128 + 16 * msd + 2 * k) * 4);
          const v4u o = {__float_as_uint(__uint_as_float(p0.x) + __uint_as_float(p1.x)), tagA, __float_as_uint(__uint_as_float(p0.y) + __uint_as_float(p1.y)), tagB};
          __builtin_amdgcn_raw_buffer_store_b128(o, rs, WS_FX + (wi * 8 + msd) * 8192 + (((xg * 2 + mat) * 4 + b) * 8 + k) * 16, 0, 16);
        }
        STAMP(12);
      }
    } else {
      const int lw = wave - 4, tl = tid - 256;
      BAR();
      {
        const u16* base = wq + (size_t)(256 * (j >> 1) + 64 * lw) * 4096 + h * 128 + 64 * (j & 1);
#pragma unroll
        for (int r = 0; r < 8; ++r) {
          const u32 row = 8 * r + (lane >> 3);
          glds16s(base, row * 8192u + (((u32)(lane & 7) ^ gsw(row)) * 16u), lds_base + L_BIG + (lw * 8 + r) * 1024);
        }
#pragma unroll
        for (int r = 0; r < 8; ++r) {
          const int row = (lw * 8 + r) * 4 + gq;
          glds16s(wuk + (size_t)(128 * j) * 4096 + h * 128, (u32)(row * 8192 + ((i16 ^ swz(row)) * 16)), lds_base + L_BIG + 32768 + (lw * 8 + r) * 1024);
        }
      }
      VMWAIT(8);
      lds_signal(L, C_T, lane);
      STAMP(1);
      __builtin_amdgcn_raw_buffer_store_b128(sv, rs, WS_INY + wid * 7168 + tl * 16, 0, 16);
      __builtin_amdgcn_raw_buffer_store_b128(sv, rs, WS_INY + wid * 7168 + (tl < 192 ? 256 + tl : tl) * 16, 0, 16);
      VMWAIT(2);
      lds_signal(L, C_T2, lane);
      VMWAIT(0);
      lds_signal(L, C_CLR, lane);
      STAMP(2);
      lds_wait(L, C_A, 4);
      while ((long long)(__builtin_amdgcn_s_memrealtime() - tk0) < WODELAY) __builtin_amdgcn_s_sleep(2);
      STAMP(3);
#pragma unroll
      for (int r = 0; r < 8; ++r) {
        const int row = (lw * 8 + r) * 4 + gq;
        glds16s(wuv + (size_t)(128 * j) * 4096 + h * 128, (u32)(row * 8192 + ((i16 ^ swz(row)) * 16)), lds_base + L_BIG + 98304 + (lw * 8 + r) * 1024);
      }
      const u16* wop = wo + (size_t)h * 128 * 512;
#pragma unroll 4
      for (int r = 0; r < 24; ++r) {
        const int row = lw * 24 + r;
        glds16s(wop + (size_t)row * 512, (u32)((lane ^ swz(row)) * 16), lds_base + L_BIG + row * 1024);
      }
      VMWAIT(24);
      lds_signal(L, C_UV, lane);
    }
    lds_wait(L, C_UV, 4);
    v4u uvf[4];
#pragma unroll
    for (int t = 0; t < 4; ++t) {
      const int r0 = 32 * t + 8 * gq + q4, r1 = r0 + 4;
      const v2u lo = trread(L + L_BIG + 98304 + r0 * 256 + (((2 * wave + (p4 >> 1)) ^ swz(r0)) * 16) + 8 * (p4 & 1));
      const v2u hi = trread(L + L_BIG + 98304 + r1 * 256 + (((2 * wave + (p4 >> 1)) ^ swz(r1)) * 16) + 8 * (p4 & 1));
      uvf[t] = v4u{lo.x, lo.y, hi.x, hi.y};
    }
    lds_signal(L, C_UVR, lane);
    STAMP(5);
    if (wave >= 4) {
      const int lw = wave - 4;
      lds_wait(L, C_UVR, 8);
      const u16* wop = wo + (size_t)h * 128 * 512;
#pragma unroll
      for (int r = 0; r < 8; ++r) {
        const int row = 96 + lw * 8 + r;
        glds16s(wop + (size_t)row * 512, (u32)((lane ^ swz(row)) * 16), lds_base + L_BIG + row * 1024);
      }
      VMWAIT(0);
      lds_signal(L, C_WO, lane);
      STAMP(6);
    }
    v4u wf[4][4];
#define READ_WF() do { \
    _Pragma("unroll") for (int t = 0; t < 4; ++t) { \
      const int r0 = 32 * t + 8 * gq + q4, r1 = r0 + 4; \
      _Pragma("unroll") for (int tt = 0; tt < 4; ++tt) { \
        const v2u lo = trread(L + L_BIG + r0 * 1024 + (((8 * wave + 2 * tt + (p4 >> 1)) ^ swz(r0)) * 16) + 8 * (p4 & 1)); \
        const v2u hi = trread(L + L_BIG + r1 * 1024 + (((8 * wave + 2 * tt + (p4 >> 1)) ^ swz(r1)) * 16) + 8 * (p4 & 1)); \
        wf[t][tt] = v4u{lo.x, lo.y, hi.x, hi.y}; \
      } \
    } } while (0)
    if (wave != 0) { lds_wait(L, C_WO, 4); READ_WF(); }
    STAMP(7);
    {
      const int b = wave >> 1, chh = wave & 1, chunk = lane & 7, srow = lane >> 3;
      const u32 src = WS_IN3 + (h * 4 + j) * 32768 + (b * 32 + srow) * 256 + chh * 128 + chunk * 16;
      if (wave == 0) {
        v4u l0 = nv, l1 = nv;
        _Pragma("unroll 1") for (u32 spins = 0; spins < SPIN_CAP; ++spins) {
          CFENCE();
          if (BADTAG(l0) != 0u) l0 = __builtin_amdgcn_raw_buffer_load_b128(rs, WS_ST3 + h * 2048 + lane * 16, 0, 16);
          if (BADTAG(l1) != 0u) l1 = __builtin_amdgcn_raw_buffer_load_b128(rs, WS_ST3 + h * 2048 + 1024 + lane * 16, 0, 16);
          if (__all((BADTAG(l0) | BADTAG(l1)) == 0u)) break;
          __builtin_amdgcn_s_sleep(2);
        }
        *(LAS u32*)(L + M_LSE + lane * 4) = l0.x;
        *(LAS u32*)(L + M_LSE + 256 + lane * 4) = l1.x;
        lds_signal(L, C_LSE, lane);
      }
      lds_wait(L, C_LSE, 1);
      STAMP(10);
      v4u v[4]; u32 lb[4];
#pragma unroll
      for (int u = 0; u < 4; ++u) { v[u] = sv; lb[u] = *(const LAS u32*)(L + M_LSE + (b * 32 + 8 * u + srow) * 4); }
      _Pragma("unroll 1") for (u32 spins = 0; spins < SPIN_CAP; ++spins) {
        CFENCE();
#pragma unroll
        for (int u = 0; u < 4; ++u) if (umax4(v[u]) == SENT) v[u] = __builtin_amdgcn_raw_buffer_load_b128(rs, src + u * 2048, 0, 16);
        u32 mxv = 0u;
#pragma unroll
        for (int u = 0; u < 4; ++u) { const u32 t = umax4(v[u]); mxv = mxv > t ? mxv : t; }
        if (__all(mxv != SENT)) break;
        __builtin_amdgcn_s_sleep(2);
      }
      STAMP(8);
      float M = fmaxf(fmaxf(__uint_as_float(lb[0]), __uint_as_float(lb[1])), fmaxf(__uint_as_float(lb[2]), __uint_as_float(lb[3])));
      M = fmaxf(M, dpp<0x128>(M));
      M = maxx32(maxx16(M));
      float ex[4];
#pragma unroll
      for (int u = 0; u < 4; ++u) ex[u] = __builtin_amdgcn_exp2f(__uint_as_float(lb[u]) - M);
      float Ls = (ex[0] + ex[1]) + (ex[2] + ex[3]);
      Ls += dpp<0x128>(Ls);
      Ls = sumx32(sumx16(Ls));
      float oc[8];
#pragma unroll
      for (int e = 0; e < 8; ++e) oc[e] = 0.f;
#pragma unroll
      for (int u = 0; u < 4; ++u) {
        const float wv = ex[u];
        oc[0] += wv * lo_f(v[u].x); oc[1] += wv * hi_f(v[u].x); oc[2] += wv * lo_f(v[u].y); oc[3] += wv * hi_f(v[u].y);
        oc[4] += wv * lo_f(v[u].z); oc[5] += wv * hi_f(v[u].z); oc[6] += wv * lo_f(v[u].w); oc[7] += wv * hi_f(v[u].w);
      }
      float r4[4], r2[2];
#pragma unroll
      for (int e = 0; e < 4; ++e) r4[e] = tred32(oc[e], oc[4 + e]);
#pragma unroll
      for (int e = 0; e < 2; ++e) r2[e] = tred16(r4[e], r4[2 + e]);
      r2[0] += dpp<0x128>(r2[0]); r2[1] += dpp<0x128>(r2[1]);
      const float iL = __builtin_amdgcn_rcpf(Ls);
      if ((lane & 8) == 0) *(LAS u32*)(L + M_OCS + b * 256 + chh * 128 + chunk * 16 + gq * 4) = pk_bf16(r2[0] * iL, r2[1] * iL);
    }
    if (wave == 0) { lds_wait(L, C_WO, 4); READ_WF(); }
    BAR();
    {
      f4 c = {0.f, 0.f, 0.f, 0.f};
#pragma unroll
      for (int t = 0; t < 4; ++t) {
        const v4u of = *(const LAS v4u*)(L + M_OCS + (i16 & 3) * 256 + (32 * t + 8 * gq) * 2);
        c = mfma16(uvf[t], of, c);
      }
      if (i16 < 4) *(LAS v2u*)(L + M_CTX + (i16 * 128 + 16 * wave + 4 * gq) * 2) = v2u{pk_bf16(c[0], c[1]), pk_bf16(c[2], c[3])};
    }
    BAR();
    {
      f4 sacc[4];
#pragma unroll
      for (int tt = 0; tt < 4; ++tt) sacc[tt] = f4{0.f, 0.f, 0.f, 0.f};
#pragma unroll
      for (int t = 0; t < 4; ++t) {
        const v4u cf = *(const LAS v4u*)(L + M_CTX + ((i16 & 3) * 128 + 32 * t + 8 * gq) * 2);
#pragma unroll
        for (int tt = 0; tt < 4; ++tt) sacc[tt] = mfma16(wf[t][tt], cf, sacc[tt]);
      }
      LAS unsigned char* priv = L + L_P + wave * 1024;
      if (i16 < 4) {
#pragma unroll
        for (int tt = 0; tt < 4; ++tt) *(LAS f4*)(priv + ((tt * 4 + i16) * 4 + gq) * 16) = sacc[tt];
      }
      const v4u o = *(const LAS v4u*)(priv + lane * 16);
      const int f = ((lane >> 2) & 3) * 32 + 4 * wave + (lane >> 4);
      __builtin_amdgcn_raw_buffer_store_b128(nosent(o), rs, WS_IN5 + (f * 128 + wid) * 64 + (lane & 3) * 16, 0, 16);
    }
    STAMP(9);
  } else {
    const int kb = xg * 16 + idx, ab = kb >> 5, as = kb & 31;
    const bool do_ffn = idx < 14;
    const int fg = idx * 8 + xg;
    const u32 sz = swz(i16);
    v4u sf[16], vf[4][4];
    {
      const u16* kvp = ckv + ((size_t)ab * 4096 + as * 128 + wave * 4) * 512;
      const u32 ldst = lds_base + L_BIG + wave * 4096;
      const u32 rb = (u32)(wave & 3) * 4u;
      __builtin_amdgcn_raw_buffer_store_b128(sv, rs, WS_IN5 + kb * 8192 + tid * 16, 0, 16);
#pragma unroll
      for (int t = 0; t < KDEPTH; ++t) {
#pragma unroll
        for (int r = 0; r < 4; ++r) glds16s(kvp + (size_t)(32 * t + r) * 512, (u32)((lane ^ swz(rb + r)) * 16), ldst + (32 * t + r) * 1024);
      }
      BAR();
#pragma unroll
      for (int t = 0; t < 4; ++t) {
        {
          const int younger = ((t + KDEPTH < 4 ? t + KDEPTH : 4) - 1 - t) * 4;
          if (younger == 12) VMWAIT(12); else if (younger == 8) VMWAIT(8); else if (younger == 4) VMWAIT(4); else VMWAIT(0);
        }
        lds_signal(L, C_KQ + t, lane);
        if (t == 3) {
#pragma unroll
          for (int e = 0; e < 4; ++e)
            __builtin_amdgcn_raw_buffer_store_b128(sv, rs, WS_IN3 + ((((8 * e + (lane >> 3)) * 4 + (wave >> 1)) * 4 + ab) * 32 + as) * 256 + (wave & 1) * 128 + (lane & 7) * 16, 0, 16);
        }
        if (t + KDEPTH < 4) {
#pragma unroll
          for (int r = 0; r < 4; ++r) glds16s(kvp + (size_t)(32 * (t + KDEPTH) + r) * 512, (u32)((lane ^ swz(rb + r)) * 16), ldst + (32 * (t + KDEPTH) + r) * 1024);
        }
        lds_wait(L, C_KQ + t, 8);
        if (t == 3) STAMP(2);
        const int k0 = 32 * t + 8 * gq + q4, k1 = k0 + 4;
#pragma unroll
        for (int tt = 0; tt < 4; ++tt) {
          const v2u lo = trread(L + L_BIG + k0 * 1024 + (((8 * wave + 2 * tt + (p4 >> 1)) ^ swz(k0)) * 16) + 8 * (p4 & 1));
          const v2u hi = trread(L + L_BIG + k1 * 1024 + (((8 * wave + 2 * tt + (p4 >> 1)) ^ swz(k1)) * 16) + 8 * (p4 & 1));
          vf[t][tt] = v4u{lo.x, lo.y, hi.x, hi.y};
        }
        if ((wave >> 1) == t) {
          LAS unsigned char* kvrow = L + L_BIG + (16 * wave + i16) * 1024;
#pragma unroll
          for (int u = 0; u < 16; ++u) sf[u] = *(const LAS v4u*)(kvrow + 256 * (u >> 2) + 16 * ((4 * (u & 3) + gq) ^ sz));
        }
        if (t == 0) lds_signal(L, C_PR, lane);
      }
    }
    STAMP(3);
    {
      const u32 src = WS_QA + (ab * 4096 + tid) * 16;
      v4u v[8];
#pragma unroll
      for (int u = 0; u < 8; ++u) v[u] = v4u{0u, tagA ^ 1u, 0u, 0u};
      _Pragma("unroll 1") for (u32 spins = 0; spins < SPIN_CAP; ++spins) {
        CFENCE();
        v[0] = __builtin_amdgcn_raw_buffer_load_b128(rs, src, 0, 16);
        if (__all(((v[0].y ^ tagA) | (v[0].w ^ tagB)) == 0u)) break;
        __builtin_amdgcn_s_sleep(2);
      }
      _Pragma("unroll 1") for (u32 spins = 0; spins < SPIN_CAP; ++spins) {
        CFENCE();
#pragma unroll
        for (int u = 1; u < 8; ++u)
          if (((v[u].y ^ tagA) | (v[u].w ^ tagB)) != 0u) v[u] = __builtin_amdgcn_raw_buffer_load_b128(rs, src, u * 8192, 16);
        u32 bad = 0u;
#pragma unroll
        for (int u = 0; u < 8; ++u) bad |= (v[u].y ^ tagA) | (v[u].w ^ tagB);
        if (__all(bad == 0u)) break;
        __builtin_amdgcn_s_sleep(1);
      }
      const int cu = tid & 127, hb = tid >> 7;
      lds_wait(L, C_PR, 8);
#pragma unroll
      for (int u = 0; u < 8; ++u) {
        const int hh = 4 * u + hb;
        *(LAS v2u*)(L + L_BIG + hh * 1024 + (((cu >> 1) ^ swz(hh)) * 16) + (cu & 1) * 8) = v2u{v[u].x, v[u].z};
      }
    }
    STAMP(4);
    VMWAIT(0);
    BAR();
    STAMP(5);
    f4 s0 = {0.f, 0.f, 0.f, 0.f}, s1 = s0;
    {
      LAS unsigned char* qrow = L + L_BIG + i16 * 1024;
      v4u qa[4], qb[4];
#pragma unroll
      for (int t = 0; t < 4; ++t) {
        const u32 off = 16 * ((4 * t + gq) ^ sz);
        qa[t] = *(const LAS v4u*)(qrow + off); qb[t] = *(const LAS v4u*)(qrow + 16384 + off);
      }
      __builtin_amdgcn_sched_barrier(0);
#pragma unroll
      for (int t = 0; t < 16; ++t) {
        s0 = mfma16(sf[t], qa[t & 3], s0);
        s1 = mfma16(sf[t], qb[t & 3], s1);
        if (t + 4 < 16) {
          const u32 off = 256 * ((t + 4) >> 2) + 16 * ((4 * (t & 3) + gq) ^ sz);
          qa[t & 3] = *(const LAS v4u*)(qrow + off); qb[t & 3] = *(const LAS v4u*)(qrow + 16384 + off);
        }
        __builtin_amdgcn_sched_barrier(0);
      }
    }
    {
      float mx0 = maxx32(maxx16(fmaxf(fmaxf(s0[0], s0[1]), fmaxf(s0[2], s0[3]))));
      float mx1 = maxx32(maxx16(fmaxf(fmaxf(s1[0], s1[1]), fmaxf(s1[2], s1[3]))));
      if (lane < 32) *(LAS float*)(L + M_WMAX + (lane * 8 + wave) * 4) = lane < 16 ? mx0 : mx1;
    }
    STAMP(6);
    v4u fu = {0u, 0u, 0u, 0u}, wd0 = fu, wd1 = fu, xv = fu, wrv = fu;
    if (do_ffn) {
      fu = __builtin_amdgcn_raw_buffer_load_b128(rs, WS_FX + fg * 8192 + tid * 16, 0, 16);
      wd0 = ldnt(wd + (size_t)(16 * fg + 2 * (lane >> 3)) * 512 + 64 * wave + 8 * (lane & 7));
      wd1 = ldnt(wd + (size_t)(16 * fg + 2 * (lane >> 3) + 1) * 512 + 64 * wave + 8 * (lane & 7));
    }
    const u16 xfin = x[ab * 512 + as * 16 + (tid & 15)];
    if (wave < 4) {
      xv = *(const GAS v4u*)(x + wave * 512 + lane * 8);
      wrv = *(const GAS v4u*)(wr + lane * 8);
    }
    __builtin_amdgcn_sched_barrier(0);
    BAR();
    float m0, m1;
    {
      const f4 a0 = *(const LAS f4*)(L + M_WMAX + i16 * 32), a1 = *(const LAS f4*)(L + M_WMAX + i16 * 32 + 16);
      const f4 b0 = *(const LAS f4*)(L + M_WMAX + 512 + i16 * 32), b1 = *(const LAS f4*)(L + M_WMAX + 512 + i16 * 32 + 16);
      m0 = fmaxf(fmaxf(fmaxf(a0[0], a0[1]), fmaxf(a0[2], a0[3])), fmaxf(fmaxf(a1[0], a1[1]), fmaxf(a1[2], a1[3])));
      m1 = fmaxf(fmaxf(fmaxf(b0[0], b0[1]), fmaxf(b0[2], b0[3])), fmaxf(fmaxf(b1[0], b1[1]), fmaxf(b1[2], b1[3])));
      const u32 pa = pk_bf16(__builtin_amdgcn_exp2f(s0[0] - m0), __builtin_amdgcn_exp2f(s0[1] - m0));
      const u32 pb = pk_bf16(__builtin_amdgcn_exp2f(s0[2] - m0), __builtin_amdgcn_exp2f(s0[3] - m0));
      const u32 pc = pk_bf16(__builtin_amdgcn_exp2f(s1[0] - m1), __builtin_amdgcn_exp2f(s1[1] - m1));
      const u32 pd = pk_bf16(__builtin_amdgcn_exp2f(s1[2] - m1), __builtin_amdgcn_exp2f(s1[3] - m1));
      const u32 poff = i16 * 256 + (((2 * wave + (gq >> 1)) ^ sz) * 16) + (gq & 1) * 8;
      *(LAS v2u*)(L + L_P + poff) = v2u{pa, pb};
      *(LAS v2u*)(L + L_P + 4096 + poff) = v2u{pc, pd};
      const float l0 = sumx32(sumx16((lo_f(pa) + hi_f(pa)) + (lo_f(pb) + hi_f(pb))));
      const float l1 = sumx32(sumx16((lo_f(pc) + hi_f(pc)) + (lo_f(pd) + hi_f(pd))));
      if (lane < 32) *(LAS float*)(L + M_WSUM + (lane * 8 + wave) * 4) = lane < 16 ? l0 : l1;
    }
    BAR();
    STAMP(7);
    if (wave == 0 && lane < 32) {
      const f4 la = *(const LAS f4*)(L + M_WSUM + lane * 32), lb = *(const LAS f4*)(L + M_WSUM + lane * 32 + 16);
      const float l = ((la[0] + la[1]) + (la[2] + la[3])) + ((lb[0] + lb[1]) + (lb[2] + lb[3]));
      const u32 lse = __float_as_uint((lane < 16 ? m0 : m1) + __builtin_amdgcn_logf(l));
      __builtin_amdgcn_raw_buffer_store_b128(v4u{lse, tagA, lse, tagB}, rs, WS_ST3 + ((lane * 4 + ab) * 32 + as) * 16, 0, 16);
    }
#pragma unroll
    for (int ht = 0; ht < 2; ++ht) {
      f4 oacc[4];
#pragma unroll
      for (int tt = 0; tt < 4; ++tt) oacc[tt] = f4{0.f, 0.f, 0.f, 0.f};
#pragma unroll
      for (int t = 0; t < 4; ++t) {
        const v4u pf = *(const LAS v4u*)(L + L_P + ht * 4096 + i16 * 256 + (((4 * t + gq) ^ sz) * 16));
#pragma unroll
        for (int tt = 0; tt < 4; ++tt) oacc[tt] = mfma16(vf[t][tt], pf, oacc[tt]);
      }
      const f4 la = *(const LAS f4*)(L + M_WSUM + ht * 512 + i16 * 32), lb = *(const LAS f4*)(L + M_WSUM + ht * 512 + i16 * 32 + 16);
      const float l = ((la[0] + la[1]) + (la[2] + la[3])) + ((lb[0] + lb[1]) + (lb[2] + lb[3]));
      const float il = __builtin_amdgcn_rcpf(l);
      const int head = 16 * ht + i16;
      LAS unsigned char* priv = L + L_BIG + 65536 + wave * 4608;
#pragma unroll
      for (int tt = 0; tt < 4; ++tt)
        *(LAS v2u*)(priv + head * 144 + tt * 32 + gq * 8) = v2u{pk_bf16(oacc[tt][0] * il, oacc[tt][1] * il), pk_bf16(oacc[tt][2] * il, oacc[tt][3] * il)};
#pragma unroll
      for (int e = 0; e < 2; ++e) {
        const int hd = 16 * ht + 8 * e + (lane >> 3);
        const v4u o = *(const LAS v4u*)(priv + hd * 144 + (lane & 7) * 16);
        __builtin_amdgcn_raw_buffer_store_b128(nosent(o), rs,
            WS_IN3 + (((hd * 4 + (wave >> 1)) * 4 + ab) * 32 + as) * 256 + (wave & 1) * 128 + (lane & 7) * 16, 0, 16);
      }
    }
    STAMP(8);
    if (wave < 4) ln_row(L, xv, wrv, wave, lane);
    if (do_ffn) {
      const int chunk = lane & 7, rg = lane >> 3;
      _Pragma("unroll 1") for (u32 spins = 0; spins < SPIN_CAP; ++spins) {
        if (__all(((fu.y ^ tagA) | (fu.w ^ tagB)) == 0u)) break;
        CFENCE();
        fu = __builtin_amdgcn_raw_buffer_load_b128(rs, WS_FX + fg * 8192 + tid * 16, 0, 16);
      }
      *(LAS v2u*)(L + L_BIG + 32768 + tid * 8) = v2u{fu.x, fu.z};
      BAR();
      if (lane < 32) {
        float g0 = 0.f, g1 = 0.f, u0 = 0.f, u1 = 0.f;
#pragma unroll
        for (int w = 0; w < 8; ++w) {
          const v2u gv = *(const LAS v2u*)(L + L_BIG + 32768 + (w * 64 + lane) * 8), uv = *(const LAS v2u*)(L + L_BIG + 32768 + (w * 64 + 32 + lane) * 8);
          g0 += __uint_as_float(gv.x); g1 += __uint_as_float(gv.y); u0 += __uint_as_float(uv.x); u1 += __uint_as_float(uv.y);
        }
        g0 = rbf(g0); g1 = rbf(g1);
        const float h0 = rbf(rbf(g0 / (1.0f + __expf(-g0))) * rbf(u0)), h1 = rbf(rbf(g1 / (1.0f + __expf(-g1))) * rbf(u1));
        *(LAS v2u*)(L + L_BIG + 40960 + wave * 256 + lane * 8) = v2u{__float_as_uint(h0), __float_as_uint(h1)};
      }
      float y[32];
#pragma unroll
      for (int b = 0; b < 4; ++b) {
        const v2u hp = *(const LAS v2u*)(L + L_BIG + 40960 + wave * 256 + (b * 16 + 2 * rg) * 4);
        const float h0 = __uint_as_float(hp.x), h1 = __uint_as_float(hp.y);
#pragma unroll
        for (int e = 0; e < 4; ++e) {
          y[b * 8 + 2 * e] = h0 * lo_f(wd0[e]) + h1 * lo_f(wd1[e]);
          y[b * 8 + 2 * e + 1] = h0 * hi_f(wd0[e]) + h1 * hi_f(wd1[e]);
        }
      }
      float n16[16], n8[8];
#pragma unroll
      for (int k = 0; k < 16; ++k) n16[k] = tred32(y[k], y[16 + k]);
#pragma unroll
      for (int k = 0; k < 8; ++k) { n8[k] = tred16(n16[k], n16[8 + k]); n8[k] += dpp<0x128>(n8[k]); }
      LAS unsigned char* priv = L + L_BIG + 65536 + wave * 4608;
      const f4 oh = (lane & 8) ? f4{n8[4], n8[5], n8[6], n8[7]} : f4{n8[0], n8[1], n8[2], n8[3]};
      *(LAS f4*)(priv + (gq * 16 + chunk * 2 + ((lane >> 3) & 1)) * 16) = oh;
      const v4u o = *(const LAS v4u*)(priv + lane * 16);
      const int f = gq * 32 + 4 * wave + (i16 >> 2);
      __builtin_amdgcn_raw_buffer_store_b128(nosent(o), rs, WS_INY + (f * 112 + fg) * 64 + (i16 & 3) * 16, 0, 16);
    }
    STAMP(9);
    float gate = 0.f;
    {
      const u32 sy = WS_INY + kb * 7168 + (tid < 448 ? tid : 0) * 16;
      v4u y0 = sv;
      __builtin_amdgcn_s_sleep(8);
      _Pragma("unroll 1") for (u32 spins = 0; spins < SPIN_CAP; ++spins) {
        CFENCE();
        if (umax4(y0) == SENT) y0 = __builtin_amdgcn_raw_buffer_load_b128(rs, sy, 0, 16);
        if (__all(umax4(y0) != SENT)) break;
        __builtin_amdgcn_s_sleep(4);
      }
      float v[4], n2[2];
#pragma unroll
      for (int e = 0; e < 4; ++e) v[e] = tid < 448 ? __uint_as_float(y0[e]) : 0.f;
#pragma unroll
      for (int e = 0; e < 2; ++e) n2[e] = tred32(v[e], v[2 + e]);
      float n1 = tred16(n2[0], n2[1]);
      n1 += dpp<0x124>(n1); n1 += dpp<0x128>(n1);
      if (i16 < 4) *(LAS float*)(L + M_FIN + (wave * 16 + i16 * 4 + gq) * 4) = n1;
      BAR();
      if (tid < 16) {
        const int m = as * 16 + tid;
        float y = 0.f;
#pragma unroll
        for (int w = 0; w < 8; ++w) y += *(const LAS float*)(L + M_FIN + (w * 16 + tid) * 4);
        const float rsv = *(const LAS float*)(L + M_RS + ab * 4);
        const float xnv = bf2f(*(const LAS u16*)(L + L_XN + (ab * 512 + m) * 2));
        y = rbf(rbf(y) * rsv);
        const float z = rbf(xnv + y);
        gate = rbf(z / (1.0f + __expf(-z)));
      }
    }
    STAMP(13);
    {
      const u32 s5 = WS_IN5 + kb * 8192 + tid * 16;
      v4u a0 = sv;
      _Pragma("unroll 1") for (u32 spins = 0; spins < SPIN_CAP; ++spins) {
        CFENCE();
        if (umax4(a0) == SENT) a0 = __builtin_amdgcn_raw_buffer_load_b128(rs, s5, 0, 16);
        if (__all(umax4(a0) != SENT)) break;
        __builtin_amdgcn_s_sleep(1);
      }
      STAMP(10);
      float n2[2];
#pragma unroll
      for (int e = 0; e < 2; ++e) n2[e] = tred32(__uint_as_float(a0[e]), __uint_as_float(a0[2 + e]));
      float n1 = tred16(n2[0], n2[1]);
      n1 += dpp<0x124>(n1); n1 += dpp<0x128>(n1);
      if (i16 < 4) *(LAS float*)(L + M_FIN + 512 + (wave * 16 + i16 * 4 + gq) * 4) = n1;
    }
    BAR();
    if (tid < 16) {
      float sacc = 0.f;
#pragma unroll
      for (int w = 0; w < 8; ++w) sacc += *(const LAS float*)(L + M_FIN + 512 + (w * 16 + tid) * 4);
      const float o = bf2f(xfin) + rbf(rbf(sacc) * gate);
      out[ab * 512 + as * 16 + tid] = __builtin_bit_cast(u16, (__bf16)o);
    }
    if (g == 0 && tid == 0) __builtin_amdgcn_raw_buffer_store_b64(v2u{tagA, tagB}, rs, WS_EPOCH, 0, 16);
  }
}

extern "C" void kernel_launch(void* const* d_in, const int* in_sizes, int n_in,
                              void* d_out, int out_size, void* d_ws, size_t ws_size,
                              hipStream_t stream) {
  (void)in_sizes; (void)n_in; (void)out_size;
  (void)hipFuncSetAttribute((const void*)mega, hipFuncAttributeMaxDynamicSharedMemorySize, LDS_BYTES);
  u64* st = (u64*)((char*)d_ws + ws_size - (1 << 20));
  mega<<<NWG, 512, LDS_BYTES, stream>>>((const u16*)d_in[0], (const u16*)d_in[1], (const u16*)d_in[2], (const u16*)d_in[3],
                                        (const u16*)d_in[4], (const u16*)d_in[5], (const u16*)d_in[7], (const u16*)d_in[8],
                                        (const u16*)d_in[9], (const u16*)d_in[10], (u16*)d_out, (unsigned char*)d_ws, st);
}
```

```cpp
#include <hip/hip_runtime.h>
#include <stdint.h>

typedef unsigned short u16;
typedef unsigned int u32;
typedef unsigned long long u64;
typedef u32 v4u __attribute__((ext_vector_type(4)));
typedef u32 v2u __attribute__((ext_vector_type(2)));
typedef float f4 __attribute__((ext_vector_type(4)));
typedef short bf16x8 __attribute__((ext_vector_type(8)));
typedef short s16x4 __attribute__((ext_vector_type(4)));
typedef __bf16 bf16x2 __attribute__((ext_vector_type(2)));
#define LAS __attribute__((address_space(3)))
#define GAS __attribute__((address_space(1)))

#define STAMPS 0

constexpr int KDEPTH = 3;
constexpr int CPF_KB = 20;
constexpr int NWG = 256;
constexpr u32 WS_EPOCH = 0;
constexpr u32 WS_QX = 4096;
constexpr u32 WS_FX = WS_QX + 32 * 8192;
constexpr u32 WS_QA = WS_FX + 112 * 8192;
constexpr u32 WS_IN3 = WS_QA + 4 * 32 * 128 * 16;
constexpr u32 WS_ST3 = WS_IN3 + 32 * 4 * 32768;
constexpr u32 WS_IN5 = WS_ST3 + 32 * 2048;
constexpr u32 WS_INY = WS_IN5 + 128 * 8192;
constexpr u32 WS_END = WS_INY + 128 * 7168;
constexpr int L_BIG = 0;
constexpr int L_XN = 131072;
constexpr int XS = 1088;
constexpr int L_P = L_XN + 4352;
constexpr int L_M = L_P + 8192;
constexpr int M_RED = L_P;
constexpr int M_QS = L_M;
constexpr int M_LSE = L_M + 1280;
constexpr int M_WMAX = L_M + 2048;
constexpr int M_WSUM = L_M + 3584;
constexpr int M_OCS = L_M + 4096;
constexpr int M_CTX = L_M + 5120;
constexpr int M_OC8 = L_M + 2048;
constexpr int M_CX8 = M_OC8 + 8 * 272;
constexpr int M_H = L_M + 6144;
constexpr int M_FIN = L_M + 6400;
constexpr int M_RS = L_M + 7424;
constexpr int M_CNT = L_M + 7488;
constexpr int L_DUMMY = L_M + 7680;
constexpr int LDS_BYTES = L_DUMMY + 1024;
enum { C_XN = 0, C_T = 1, C_RED = 2, C_CLR = 3, C_KV = 4, C_A = 5, C_UV = 6, C_UVR = 7, C_F = 8, C_FR = 9, C_H = 10, C_T2 = 11, C_QS = 12, C_LSE = 13, C_FA = 14, C_PR = 15, C_KQ = 16  , C_WO = 20 };
constexpr u32 SENT = 0xFFFFFFFFu;
constexpr u32 SPIN_CAP = 1u << 16;

__device__ __forceinline__ float bf2f(u32 v) { return __uint_as_float(v << 16); }
__device__ __forceinline__ float lo_f(u32 v) { return __uint_as_float(v << 16); }
__device__ __forceinline__ float hi_f(u32 v) { return __uint_as_float(v & 0xffff0000u); }
__device__ __forceinline__ u32 pk_bf16(float a, float b) {
  bf16x2 r; r[0] = (__bf16)a; r[1] = (__bf16)b; return __builtin_bit_cast(u32, r);
}
__device__ __forceinline__ float rbf(float a) { return (float)((__bf16)a); }
__device__ __forceinline__ f4 mfma16(v4u a, v4u b, f4 c) {
  return __builtin_amdgcn_mfma_f32_16x16x32_bf16(__builtin_bit_cast(bf16x8, a), __builtin_bit_cast(bf16x8, b), c, 0, 0, 0);
}
__device__ __forceinline__ float dot2(u32 a, u32 b, float c) {
  return __builtin_amdgcn_fdot2_f32_bf16(__builtin_bit_cast(bf16x2, a), __builtin_bit_cast(bf16x2, b), c, false);
}
__device__ __forceinline__ u32 swz(u32 row) { return ((((row >> 3) ^ (row >> 2)) & 1u) << 3) | ((row & 3u) << 1) | ((row >> 3) & 1u); }
__device__ __forceinline__ v2u trread(LAS unsigned char* p) {
  return __builtin_bit_cast(v2u, __builtin_amdgcn_ds_read_tr16_b64_v4i16((LAS s16x4*)p));
}
__device__ __forceinline__ void glds16s(const void* sbase, u32 voff, u32 lds_dst) {
  u32 keep;
  asm volatile("s_mov_b32 %0, m0\n\ts_mov_b32 m0, %3\n\ts_nop 0\n\tglobal_load_lds_dwordx4 %1, %2 nt\n\ts_mov_b32 m0, %0"
               : "=&s"(keep) : "v"(voff), "s"(sbase), "s"(lds_dst) : "memory");
}
__device__ __forceinline__ void glds16v(const void* gsrc, u32 lds_dst) {
  u32 keep;
  asm volatile("s_mov_b32 %0, m0\n\ts_mov_b32 m0, %2\n\ts_nop 0\n\tglobal_load_lds_dwordx4 %1, off nt\n\ts_mov_b32 m0, %0"
               : "=&s"(keep) : "v"(gsrc), "s"(lds_dst) : "memory");
}
__device__ __forceinline__ v4u ldnt(const void* p) { return __builtin_nontemporal_load((const GAS v4u*)p); }
template <int CTRL> __device__ __forceinline__ float dpp(float v) {
  return __int_as_float(__builtin_amdgcn_update_dpp(0, __float_as_int(v), CTRL, 0xf, 0xf, true));
}
__device__ __forceinline__ float sum16(float v) { v += dpp<0xB1>(v); v += dpp<0x4E>(v); v += dpp<0x141>(v); v += dpp<0x140>(v); return v; }
__device__ __forceinline__ float sumx16(float v) { auto r = __builtin_amdgcn_permlane16_swap(__float_as_uint(v), __float_as_uint(v), false, false); return __uint_as_float(r[0]) + __uint_as_float(r[1]); }
__device__ __forceinline__ float sumx32(float v) { auto r = __builtin_amdgcn_permlane32_swap(__float_as_uint(v), __float_as_uint(v), false, false); return __uint_as_float(r[0]) + __uint_as_float(r[1]); }
__device__ __forceinline__ float maxx16(float v) { auto r = __builtin_amdgcn_permlane16_swap(__float_as_uint(v), __float_as_uint(v), false, false); return fmaxf(__uint_as_float(r[0]), __uint_as_float(r[1])); }
__device__ __forceinline__ float maxx32(float v) { auto r = __builtin_amdgcn_permlane32_swap(__float_as_uint(v), __float_as_uint(v), false, false); return fmaxf(__uint_as_float(r[0]), __uint_as_float(r[1])); }
__device__ __forceinline__ float sum64(float v) { return sumx32(sumx16(sum16(v))); }
__device__ __forceinline__ float tred32(float a, float b) { auto r = __builtin_amdgcn_permlane32_swap(__float_as_uint(a), __float_as_uint(b), false, false); return __uint_as_float(r[0]) + __uint_as_float(r[1]); }
__device__ __forceinline__ float tred16(float a, float b) { auto r = __builtin_amdgcn_permlane16_swap(__float_as_uint(a), __float_as_uint(b), false, false); return __uint_as_float(r[0]) + __uint_as_float(r[1]); }

#define BAR() do { asm volatile("s_waitcnt lgkmcnt(0)" ::: "memory"); __builtin_amdgcn_s_barrier(); asm volatile("" ::: "memory"); } while (0)
#define BADTAG(v) (((v).y ^ tagA) | ((v).w ^ tagB))
#define CFENCE() asm volatile("" ::: "memory")
#define VMWAIT(n) asm volatile("s_waitcnt vmcnt(" #n ")" ::: "memory")
__device__ __forceinline__ u32 umax4(v4u v) { u32 a = v.x > v.y ? v.x : v.y, b = v.z > v.w ? v.z : v.w; return a > b ? a : b; }
__device__ __forceinline__ v4u nosent(v4u v) {
  v.x = v.x < 0xFFFFFFFEu ? v.x : 0xFFFFFFFEu; v.y = v.y < 0xFFFFFFFEu ? v.y : 0xFFFFFFFEu;
  v.z = v.z < 0xFFFFFFFEu ? v.z : 0xFFFFFFFEu; v.w = v.w < 0xFFFFFFFEu ? v.w : 0xFFFFFFFEu; return v;
}
__device__ __forceinline__ void lds_signal(LAS unsigned char* L, int c, int lane) {
  asm volatile("s_waitcnt lgkmcnt(0)" ::: "memory");
  if (lane == 0) __hip_atomic_fetch_add((LAS u32*)(L + M_CNT) + c, 1u, __ATOMIC_RELAXED, __HIP_MEMORY_SCOPE_WORKGROUP);
}
__device__ __forceinline__ void lds_wait(LAS unsigned char* L, int c, u32 target) {
  _Pragma("unroll 1") for (u32 spins = 0; spins < (1u << 22); ++spins) {
    if (*((volatile LAS u32*)(L + M_CNT) + c) >= target) break;
    __builtin_amdgcn_s_sleep(0);
  }
  CFENCE();
}

__device__ __forceinline__ void ln_row(LAS unsigned char* L, v4u xv, v4u wrv, int r, int lane) {
  float xf[8] = {lo_f(xv.x), hi_f(xv.x), lo_f(xv.y), hi_f(xv.y), lo_f(xv.z), hi_f(xv.z), lo_f(xv.w), hi_f(xv.w)};
  float s = ((xf[0] + xf[1]) + (xf[2] + xf[3])) + ((xf[4] + xf[5]) + (xf[6] + xf[7]));
  const float mean = sum64(s) * (1.0f / 512.0f);
  float q2 = 0.f;
#pragma unroll
  for (int e = 0; e < 8; ++e) { const float d = xf[e] - mean; q2 += d * d; }
  const float var = rbf(sum64(q2) * (1.0f / 512.0f));
  const float mb = rbf(mean);
  const float den = rbf(sqrtf(rbf(var + 1e-5f)));
  float xn[8];
#pragma unroll
  for (int e = 0; e < 8; ++e) xn[e] = rbf(xf[e] - mb) / den;
  v4u xnp = {pk_bf16(xn[0], xn[1]), pk_bf16(xn[2], xn[3]), pk_bf16(xn[4], xn[5]), pk_bf16(xn[6], xn[7])};
  *(LAS v4u*)(L + L_XN + r * XS + lane * 16) = xnp;
  lds_signal(L, C_XN, lane);
  float rr = dot2(xnp.x, wrv.x, 0.f); rr = dot2(xnp.y, wrv.y, rr); rr = dot2(xnp.z, wrv.z, rr); rr = dot2(xnp.w, wrv.w, rr);
  rr = rbf(sum64(rr));
  if (lane == 0) *(LAS float*)(L + M_RS + r * 4) = rbf(1.0f / (1.0f + __expf(-rr)));
}

__device__ __forceinline__ u32 gsw(u32 row) { return (((row >> 1) & 1u) | (((row >> 3) & 1u) << 1)) << 1; }
__device__ __forceinline__ void tile_gemv(LAS unsigned char* L, int tile_off, int m0, int kq, int lane) {
  const int i16 = lane & 15, gq = lane >> 4, q4 = i16 >> 2, p4 = i16 & 3;
  f4 acc[4];
#pragma unroll
  for (int dt = 0; dt < 4; ++dt) acc[dt] = f4{0.f, 0.f, 0.f, 0.f};
  const u32 fl = gsw(8 * gq + q4);
#pragma unroll
  for (int t = 0; t < 2; ++t) {
    const int r0 = 64 * kq + 32 * t + 8 * gq + q4;
    const v4u xf = *(const LAS v4u*)(L + L_XN + (i16 & 3) * XS + (m0 + 64 * kq + 32 * t + 8 * gq) * 2);
    LAS unsigned char* b0 = L + tile_off + r0 * 128 + 8 * (p4 & 1);
#pragma unroll
    for (int dt = 0; dt < 4; ++dt) {
      const v2u a0 = trread(b0 + (((2 * dt + (p4 >> 1)) ^ fl) * 16)), a1 = trread(b0 + 512 + (((2 * dt + (p4 >> 1)) ^ fl) * 16));
      acc[dt] = mfma16(v4u{a0.x, a0.y, a1.x, a1.y}, xf, acc[dt]);
    }
  }
  if (i16 < 4) {
#pragma unroll
    for (int dt = 0; dt < 4; ++dt) *(LAS f4*)(L + M_RED + (((kq * 4 + dt) * 4 + gq) * 4 + i16) * 16) = acc[dt];
  }
}

__global__ __launch_bounds__(512, 2) void mega(const u16* __restrict__ x, const u16* __restrict__ ckv, const u16* __restrict__ wq,
                                               const u16* __restrict__ wuk, const u16* __restrict__ wuv, const u16* __restrict__ wo,
                                               const u16* __restrict__ wr, const u16* __restrict__ wg, const u16* __restrict__ wu,
                                               const u16* __restrict__ wd, u16* __restrict__ out, unsigned char* ws, u64* stamps) {
  extern __shared__ __attribute__((aligned(16))) unsigned char lds_raw[];
  LAS unsigned char* L = (LAS unsigned char*)lds_raw;
  const int tid = threadIdx.x, lane = tid & 63;
  const int wave = __builtin_amdgcn_readfirstlane(tid >> 6);
  const int g = blockIdx.x, xg = g & 7, idx = g >> 3;
  const int i16 = lane & 15, gq = lane >> 4, q4 = i16 >> 2, p4 = i16 & 3;
  const __amdgpu_buffer_rsrc_t rs = __builtin_amdgcn_make_buffer_rsrc((void*)ws, 0, (int)WS_END, 0x00020000);
  const u32 lds_base = (u32)(size_t)lds_raw;
  const v4u sv = {SENT, SENT, SENT, SENT};
#define STAMP(k)
  asm volatile("" :: "s"(x), "s"(ckv), "s"(wq), "s"(wuk), "s"(wuv), "s"(wo), "s"(wr), "s"(wg), "s"(wu), "s"(wd), "s"(out), "s"(ws));
  {
    u64 pc;
    asm volatile("s_getpc_b64 %0" : "=s"(pc));
    const int slice = idx;
    if (wave == 3 && slice < CPF_KB) {
      u32 keep;
      asm volatile("s_mov_b32 %0, m0\n\ts_mov_b32 m0, %3\n\ts_nop 0\n\tglobal_load_lds_dwordx4 %1, %2\n\ts_mov_b32 m0, %0"
                   : "=&s"(keep) : "v"((u32)(slice * 1024 + lane * 16)), "s"(pc & ~(u64)63), "s"(lds_base + L_DUMMY) : "memory");
    }
  }
  STAMP(0);
  if (tid < 24) *((LAS u32*)(L + M_CNT) + tid) = 0u;
  u32 tagA = 0u, tagB = 0u;
#define READ_EPOCH() do { \
    v2u ep_; \
    asm volatile("s_load_dwordx2 %0, %1, 0x0\n\ts_waitcnt lgkmcnt(0)" : "=s"(ep_) : "s"(ws) : "memory"); \
    tagA = ep_.x + 1u; tagB = ep_.y + 1u; } while (0)
#define nv (v4u{0u, tagA ^ 1u, 0u, 0u})
  if (idx >= 16) {
    const int wi = idx - 16, h = xg * 4 + (wi >> 2), j = wi & 3, wid = xg * 16 + wi;
    if (wave < 4) {
      const int a = wave;
      const v4u xv = *(const GAS v4u*)(x + a * 512 + lane * 8);
      const v4u wrv = *(const GAS v4u*)(wr + lane * 8);
      BAR();
      ln_row(L, xv, wrv, a, lane);
      READ_EPOCH();
      if (lane < 32) {
        __builtin_amdgcn_raw_buffer_store_b128(nv, rs, WS_QX + (((h * 4 + a) * 2 + (j >> 1)) * 64 + 32 * (j & 1) + lane) * 16, 0, 16);
        __builtin_amdgcn_raw_buffer_store_b128(nv, rs, WS_QA + (((lane >> 3) * 32 + h) * 128 + 32 * j + 8 * a + (lane & 7)) * 16, 0, 16);
      }
      lds_wait(L, C_XN, 4);
      lds_wait(L, C_T, 4);
      STAMP(1);
      tile_gemv(L, L_BIG, 256 * (j >> 1), a, lane);
      lds_signal(L, C_RED, lane);
      lds_wait(L, C_RED, 4);
      if (lane < 32) {
        float s0 = 0.f, s1 = 0.f;
#pragma unroll
        for (int kq = 0; kq < 4; ++kq) {
          const v2u pv = *(const LAS v2u*)(L + M_RED + ((kq * 16 + (lane >> 1)) * 4 + a) * 16 + (lane & 1) * 8);
          s0 += __uint_as_float(pv.x); s1 += __uint_as_float(pv.y);
        }
        __builtin_amdgcn_raw_buffer_store_b128(v4u{__float_as_uint(s0), tagA, __float_as_uint(s1), tagB}, rs,
                                               WS_QX + (((h * 4 + a) * 2 + (j >> 1)) * 64 + 32 * (j & 1) + lane) * 16, 0, 16);
      }
      STAMP(2);
      lds_wait(L, C_T2, 4);
      v4u wukf[2][4];
#pragma unroll
      for (int ct = 0; ct < 2; ++ct)
#pragma unroll
        for (int t = 0; t < 4; ++t)
          wukf[ct][t] = *(const LAS v4u*)(L + L_BIG + 32768 + (32 * a + 16 * ct + i16) * 256 + (((4 * t + gq) ^ swz(i16)) * 16));
      v4u qu[2];
      qu[0] = qu[1] = v4u{0u, tagA ^ 1u, 0u, 0u};
      const u32 qsrc = WS_QX + ((h * 4 + a) * 128 + lane) * 16;
      _Pragma("unroll 1") for (u32 spins = 0; spins < SPIN_CAP; ++spins) {
        CFENCE();
#pragma unroll
        for (int u = 0; u < 2; ++u)
          if (((qu[u].y ^ tagA) | (qu[u].w ^ tagB)) != 0u) qu[u] = __builtin_amdgcn_raw_buffer_load_b128(rs, qsrc + u * 1024, 0, 16);
        const u32 bad = (qu[0].y ^ tagA) | (qu[0].w ^ tagB) | (qu[1].y ^ tagA) | (qu[1].w ^ tagB);
        if (__all(bad == 0u)) break;
      }
      *(LAS u32*)(L + M_QS + a * 320 + lane * 4) = pk_bf16(__uint_as_float(qu[0].x) + __uint_as_float(qu[1].x), __uint_as_float(qu[0].z) + __uint_as_float(qu[1].z));
      lds_signal(L, C_QS, lane);
      lds_wait(L, C_QS, 4);
      STAMP(3);
      LAS unsigned char* priv = L + L_BIG + 65536 + a * 2048;
      f4 qa0 = {0.f, 0.f, 0.f, 0.f}, qa1 = qa0;
#pragma unroll
      for (int t = 0; t < 4; ++t) {
        const v4u qf = *(const LAS v4u*)(L + M_QS + (i16 & 3) * 320 + (32 * t + 8 * gq) * 2);
        qa0 = mfma16(wukf[0][t], qf, qa0);
        qa1 = mfma16(wukf[1][t], qf, qa1);
      }
      const float SC = 0.08838834764831845f * 1.4426950408889634f;
      if (i16 < 4) {
        *(LAS v2u*)(priv + 1024 + (i16 * 8 + gq) * 8) = v2u{pk_bf16(qa0[0] * SC, qa0[1] * SC), pk_bf16(qa0[2] * SC, qa0[3] * SC)};
        *(LAS v2u*)(priv + 1024 + (i16 * 8 + 4 + gq) * 8) = v2u{pk_bf16(qa1[0] * SC, qa1[1] * SC), pk_bf16(qa1[2] * SC, qa1[3] * SC)};
      }
      lds_wait(L, C_CLR, 4);
      if (lane < 32) {
        const v2u pv = *(const LAS v2u*)(priv + 1024 + lane * 8);
        __builtin_amdgcn_raw_buffer_store_b128(v4u{pv.x, tagA, pv.y, tagB}, rs, WS_QA + (((lane >> 3) * 32 + h) * 128 + 32 * j + 8 * a + (lane & 7)) * 16, 0, 16);
      }
      lds_signal(L, C_A, lane);
      STAMP(4);
      if (wi < 14) {
        v4u fw[8];
        const int mat = wave & 1, ks = wave >> 1;
        const u16* src = (mat ? wu : wg) + (size_t)(64 * xg + 32 * ks + 8 * gq) * 1792 + 128 * wi + 8 * i16;
#pragma unroll
        for (int jj = 0; jj < 8; ++jj) fw[jj] = ldnt(src + (size_t)jj * 1792);
        const v4u af = *(const LAS v4u*)(L + L_XN + (i16 >> 2) * XS + (64 * xg + 32 * ks + 8 * gq) * 2);
        float pr[8];
#pragma unroll
        for (int cc = 0; cc < 8; ++cc) {
          const u32 sel = (cc & 1) ? 0x07060302u : 0x05040100u;
          v4u bfr;
          bfr.x = __builtin_amdgcn_perm(fw[1][cc >> 1], fw[0][cc >> 1], sel);
          bfr.y = __builtin_amdgcn_perm(fw[3][cc >> 1], fw[2][cc >> 1], sel);
          bfr.z = __builtin_amdgcn_perm(fw[5][cc >> 1], fw[4][cc >> 1], sel);
          bfr.w = __builtin_amdgcn_perm(fw[7][cc >> 1], fw[6][cc >> 1], sel);
          pr[cc] = mfma16(af, bfr, f4{0.f, 0.f, 0.f, 0.f})[0];
        }
        LAS f4* dst = (LAS f4*)(L + M_RED + ((wave * 4 + gq) * 128 + 8 * i16) * 4);
        dst[0] = f4{pr[0], pr[1], pr[2], pr[3]};
        dst[1] = f4{pr[4], pr[5], pr[6], pr[7]};
        lds_signal(L, C_FA, lane);
        lds_wait(L, C_FA, 4);
#pragma unroll
        for (int u = 0; u < 2; ++u) {
          const int U = ks * 128 + u * 64 + lane, msd = U >> 5, b = (U >> 3) & 3, k = U & 7;
          const v2u p0 = *(const LAS v2u*)(L + M_RED + (((0 + mat) * 4 + b) * 128 + 16 * msd + 2 * k) * 4);
          const v2u p1 = *(const LAS v2u*)(L + M_RED + (((2 + mat) * 4 + b) * 128 + 16 * msd + 2 * k) * 4);
          const v4u o = {__float_as_uint(__uint_as_float(p0.x) + __uint_as_float(p1.x)), tagA, __float_as_uint(__uint_as_float(p0.y) + __uint_as_float(p1.y)), tagB};
          __builtin_amdgcn_raw_buffer_store_b128(o, rs, WS_FX + (wi * 8 + msd) * 8192 + (((xg * 2 + mat) * 4 + b) * 8 + k) * 16, 0, 16);
        }
        STAMP(12);
      }
    } else {
      const int lw = wave - 4, tl = tid - 256;
      BAR();
      {
        const u16* base = wq + (size_t)(256 * (j >> 1) + 64 * lw) * 4096 + h * 128 + 64 * (j & 1);
#pragma unroll
        for (int r = 0; r < 8; ++r) {
          const u32 row = 8 * r + (lane >> 3);
          glds16s(base, row * 8192u + (((u32)(lane & 7) ^ gsw(row)) * 16u), lds_base + L_BIG + (lw * 8 + r) * 1024);
        }
#pragma unroll
        for (int r = 0; r < 8; ++r) {
          const int row = (lw * 8 + r) * 4 + gq;
          glds16s(wuk + (size_t)(128 * j) * 4096 + h * 128, (u32)(row * 8192 + ((i16 ^ swz(row)) * 16)), lds_base + L_BIG + 32768 + (lw * 8 + r) * 1024);
        }
      }
      VMWAIT(8);
      lds_signal(L, C_T, lane);
      STAMP(1);
      __builtin_amdgcn_raw_buffer_store_b128(sv, rs, WS_INY + wid * 7168 + tl * 16, 0, 16);
      __builtin_amdgcn_raw_buffer_store_b128(sv, rs, WS_INY + wid * 7168 + (tl < 192 ? 256 + tl : tl) * 16, 0, 16);
      VMWAIT(2);
      lds_signal(L, C_T2, lane);
      VMWAIT(0);
      lds_signal(L, C_CLR, lane);
      STAMP(2);
      lds_wait(L, C_A, 4);
      STAMP(3);
#pragma unroll
      for (int r = 0; r < 8; ++r) {
        const int row = (lw * 8 + r) * 4 + gq;
        glds16s(wuv + (size_t)(128 * j) * 4096 + h * 128, (u32)(row * 8192 + ((i16 ^ swz(row)) * 16)), lds_base + L_BIG + 98304 + (lw * 8 + r) * 1024);
      }
      const u16* wop = wo + (size_t)h * 128 * 512;
#pragma unroll 4
      for (int r = 0; r < 24; ++r) {
        const int row = lw * 24 + r;
        glds16s(wop + (size_t)row * 512, (u32)((lane ^ swz(row)) * 16), lds_base + L_BIG + row * 1024);
      }
      VMWAIT(24);
      lds_signal(L, C_UV, lane);
    }
    lds_wait(L, C_UV, 4);
    v4u uvf[4];
#pragma unroll
    for (int t = 0; t < 4; ++t) {
      const int r0 = 32 * t + 8 * gq + q4, r1 = r0 + 4;
      const v2u lo = trread(L + L_BIG + 98304 + r0 * 256 + (((2 * wave + (p4 >> 1)) ^ swz(r0)) * 16) + 8 * (p4 & 1));
      const v2u hi = trread(L + L_BIG + 98304 + r1 * 256 + (((2 * wave + (p4 >> 1)) ^ swz(r1)) * 16) + 8 * (p4 & 1));
      uvf[t] = v4u{lo.x, lo.y, hi.x, hi.y};
    }
    lds_signal(L, C_UVR, lane);
    STAMP(5);
    if (wave >= 4) {
      const int lw = wave - 4;
      lds_wait(L, C_UVR, 8);
      const u16* wop = wo + (size_t)h * 128 * 512;
#pragma unroll
      for (int r = 0; r < 8; ++r) {
        const int row = 96 + lw * 8 + r;
        glds16s(wop + (size_t)row * 512, (u32)((lane ^ swz(row)) * 16), lds_base + L_BIG + row * 1024);
      }
      VMWAIT(0);
      lds_signal(L, C_WO, lane);
      STAMP(6);
    }
    v4u wf[4][4];
#define READ_WF() do { \
    _Pragma("unroll") for (int t = 0; t < 4; ++t) { \
      const int r0 = 32 * t + 8 * gq + q4, r1 = r0 + 4; \
      _Pragma("unroll") for (int tt = 0; tt < 4; ++tt) { \
        const v2u lo = trread(L + L_BIG + r0 * 1024 + (((8 * wave + 2 * tt + (p4 >> 1)) ^ swz(r0)) * 16) + 8 * (p4 & 1)); \
        const v2u hi = trread(L + L_BIG + r1 * 1024 + (((8 * wave + 2 * tt + (p4 >> 1)) ^ swz(r1)) * 16) + 8 * (p4 & 1)); \
        wf[t][tt] = v4u{lo.x, lo.y, hi.x, hi.y}; \
      } \
    } } while (0)
    bool have_wf = false;
    if (wave != 0) {
      _Pragma("unroll 1") for (u32 spins = 0; spins < (1u << 22); ++spins) {
        if (*((volatile LAS u32*)(L + M_CNT) + C_WO) >= 4u) { have_wf = true; break; }
        if (*((volatile LAS u32*)(L + M_CNT) + C_LSE) >= 1u) break;
        __builtin_amdgcn_s_sleep(0);
      }
      CFENCE();
      if (have_wf) READ_WF();
    }
    STAMP(7);
    {
      const int b = wave >> 1, chh = wave & 1, chunk = lane & 7, srow = lane >> 3;
      const u32 src = WS_IN3 + (h * 4 + j) * 32768 + (b * 32 + srow) * 256 + chh * 128 + chunk * 16;
      if (wave == 0) {
        v4u l0 = nv, l1 = nv;
        _Pragma("unroll 1") for (u32 spins = 0; spins < SPIN_CAP; ++spins) {
          CFENCE();
          if (BADTAG(l0) != 0u) l0 = __builtin_amdgcn_raw_buffer_load_b128(rs, WS_ST3 + h * 2048 + lane * 16, 0, 16);
          if (BADTAG(l1) != 0u) l1 = __builtin_amdgcn_raw_buffer_load_b128(rs, WS_ST3 + h * 2048 + 1024 + lane * 16, 0, 16);
          if (__all((BADTAG(l0) | BADTAG(l1)) == 0u)) break;
          __builtin_amdgcn_s_sleep(2);
        }
        *(LAS u32*)(L + M_LSE + lane * 4) = l0.x;
        *(LAS u32*)(L + M_LSE + 256 + lane * 4) = l1.x;
        lds_signal(L, C_LSE, lane);
      }
      lds_wait(L, C_LSE, 1);
      STAMP(10);
      v4u v[4]; float lb[4];
#pragma unroll
      for (int u = 0; u < 4; ++u) v[u] = __builtin_amdgcn_raw_buffer_load_b128(rs, src + u * 2048, 0, 16);
      __builtin_amdgcn_sched_barrier(0);
#pragma unroll
      for (int u = 0; u < 4; ++u) lb[u] = *(const LAS float*)(L + M_LSE + (b * 32 + 8 * u + srow) * 4);
      float M = fmaxf(fmaxf(lb[0], lb[1]), fmaxf(lb[2], lb[3]));
      M = fmaxf(M, dpp<0x128>(M));
      M = maxx32(maxx16(M));
      float ex[4];
#pragma unroll
      for (int u = 0; u < 4; ++u) ex[u] = __builtin_amdgcn_exp2f(lb[u] - M);
      float Ls = (ex[0] + ex[1]) + (ex[2] + ex[3]);
      Ls += dpp<0x128>(Ls);
      Ls = sumx32(sumx16(Ls));
      const float iL = __builtin_amdgcn_rcpf(Ls);
#pragma unroll
      for (int u = 0; u < 4; ++u) ex[u] *= iL;
      if (!have_wf && *((volatile LAS u32*)(L + M_CNT) + C_WO) >= 4u) { CFENCE(); READ_WF(); have_wf = true; }
      __builtin_amdgcn_sched_barrier(0);
      _Pragma("unroll 1") for (u32 spins = 0; spins < SPIN_CAP; ++spins) {
        u32 mxv = 0u;
#pragma unroll
        for (int u = 0; u < 4; ++u) { const u32 t = umax4(v[u]); mxv = mxv > t ? mxv : t; }
        if (__all(mxv != SENT)) break;
        __builtin_amdgcn_s_sleep(2);
        CFENCE();
#pragma unroll
        for (int u = 0; u < 4; ++u) if (umax4(v[u]) == SENT) v[u] = __builtin_amdgcn_raw_buffer_load_b128(rs, src + u * 2048, 0, 16);
      }
      STAMP(8);
      float oc[8];
#pragma unroll
      for (int e = 0; e < 8; ++e) oc[e] = 0.f;
#pragma unroll
      for (int u = 0; u < 4; ++u) {
        const float wv = ex[u];
        oc[0] += wv * lo_f(v[u].x); oc[1] += wv * hi_f(v[u].x); oc[2] += wv * lo_f(v[u].y); oc[3] += wv * hi_f(v[u].y);
        oc[4] += wv * lo_f(v[u].z); oc[5] += wv * hi_f(v[u].z); oc[6] += wv * lo_f(v[u].w); oc[7] += wv * hi_f(v[u].w);
      }
      float r4[4], r2[2];
#pragma unroll
      for (int e = 0; e < 4; ++e) r4[e] = tred32(oc[e], oc[4 + e]);
#pragma unroll
      for (int e = 0; e < 2; ++e) r2[e] = tred16(r4[e], r4[2 + e]);
      r2[0] += dpp<0x128>(r2[0]); r2[1] += dpp<0x128>(r2[1]);
      if ((lane & 8) == 0) {
        const u32 hp = pk_bf16(r2[0], r2[1]);
        *(LAS u32*)(L + M_OC8 + b * 272 + chh * 128 + chunk * 16 + gq * 4) = hp;
        *(LAS u32*)(L + M_OC8 + (4 + b) * 272 + chh * 128 + chunk * 16 + gq * 4) = pk_bf16(r2[0] - lo_f(hp), r2[1] - hi_f(hp));
      }
    }
    BAR();
    {
      f4 c = {0.f, 0.f, 0.f, 0.f};
#pragma unroll
      for (int t = 0; t < 4; ++t) {
        const v4u of = *(const LAS v4u*)(L + M_OC8 + (i16 & 7) * 272 + (32 * t + 8 * gq) * 2);
        c = mfma16(uvf[t], of, c);
      }
#pragma unroll
      for (int e = 0; e < 4; ++e) c[e] += dpp<0x104>(c[e]);
      if (i16 < 4) {
        const u32 h0 = pk_bf16(c[0], c[1]), h1 = pk_bf16(c[2], c[3]);
        *(LAS v2u*)(L + M_CX8 + i16 * 272 + (16 * wave + 4 * gq) * 2) = v2u{h0, h1};
        *(LAS v2u*)(L + M_CX8 + (4 + i16) * 272 + (16 * wave + 4 * gq) * 2) = v2u{pk_bf16(c[0] - lo_f(h0), c[1] - hi_f(h0)), pk_bf16(c[2] - lo_f(h1), c[3] - hi_f(h1))};
      }
    }
    if (!have_wf) READ_WF();
    BAR();
    {
      f4 sacc[4];
#pragma unroll
      for (int tt = 0; tt < 4; ++tt) sacc[tt] = f4{0.f, 0.f, 0.f, 0.f};
#pragma unroll
      for (int t = 0; t < 4; ++t) {
        const v4u cf = *(const LAS v4u*)(L + M_CX8 + (i16 & 7) * 272 + (32 * t + 8 * gq) * 2);
#pragma unroll
        for (int tt = 0; tt < 4; ++tt) sacc[tt] = mfma16(wf[t][tt], cf, sacc[tt]);
      }

      LAS unsigned char* priv = L + L_BIG + wave * 2048;
      if (i16 < 8) {
#pragma unroll
        for (int tt = 0; tt < 4; ++tt) *(LAS f4*)(priv + (i16 >> 2) * 1024 + ((tt * 4 + (i16 & 3)) * 4 + gq) * 16) = sacc[tt];
      }
      const v4u o = __builtin_bit_cast(v4u, *(const LAS f4*)(priv + lane * 16) + *(const LAS f4*)(priv + 1024 + lane * 16));
      const int f = ((lane >> 2) & 3) * 32 + 4 * wave + (lane >> 4);
      __builtin_amdgcn_raw_buffer_store_b128(nosent(o), rs, WS_IN5 + (f * 128 + wid) * 64 + (lane & 3) * 16, 0, 16);
    }
    STAMP(9);
  } else {
    const int kb = xg * 16 + idx, ab = kb >> 5, as = kb & 31;
    const bool do_ffn = idx < 14;
    const int fg = idx * 8 + xg;
    const u32 sz = swz(i16);
    v4u sf[16], vf[4][4], qv[8];
    {
      const u16* kvp = ckv + ((size_t)ab * 4096 + as * 128 + (wave - 4) * 8) * 512;
      const u32 ldst = lds_base + L_BIG + (wave - 4) * 8192;
      const u32 rb = (u32)(wave & 1) * 8u;
      if (wave < 4) {
        const int ph = xg * 4 + (idx >> 2), pj = idx & 3;
        const u16* base = wq + (size_t)(256 * (pj >> 1) + 64 * wave) * 4096 + ph * 128 + 64 * (pj & 1);
#pragma unroll
        for (int r = 4; r < 8; ++r) glds16s(base, (u32)(8 * r + (lane >> 3)) * 8192u + (u32)(lane & 7) * 16u, lds_base + L_DUMMY);
#pragma unroll
        for (int r = 4; r < 8; ++r) glds16s(wuk + (size_t)(128 * pj) * 4096 + ph * 128, (u32)(((wave * 8 + r) * 4 + gq) * 8192 + i16 * 16), lds_base + L_DUMMY);
        __builtin_amdgcn_raw_buffer_store_b128(sv, rs, WS_IN5 + kb * 8192 + tid * 16, 0, 16);
        __builtin_amdgcn_raw_buffer_store_b128(sv, rs, WS_IN5 + kb * 8192 + tid * 16, 4096, 16);
      } else {
#pragma unroll
        for (int t = 0; t < KDEPTH; ++t) {
#pragma unroll
          for (int r = 0; r < 8; ++r) glds16s(kvp + (size_t)(32 * t + r) * 512, (u32)((lane ^ swz(rb + r)) * 16), ldst + (32 * t + r) * 1024);
        }
      }
      READ_EPOCH();
      if (wave == 0 && lane < 32) __builtin_amdgcn_raw_buffer_store_b128(nv, rs, WS_ST3 + ((lane * 4 + ab) * 32 + as) * 16, 0, 16);
      BAR();
      if (wave < 4) ln_row(L, *(const GAS v4u*)(x + wave * 512 + lane * 8), *(const GAS v4u*)(wr + lane * 8), wave, lane);
#pragma unroll
      for (int t = 0; t < 4; ++t) {
        if (wave >= 4) {
          const int younger = ((t + KDEPTH < 4 ? t + KDEPTH : 4) - 1 - t) * 8;
          if (younger == 24) VMWAIT(24); else if (younger == 16) VMWAIT(16); else if (younger == 8) VMWAIT(8); else VMWAIT(0);
          lds_signal(L, C_KQ + t, lane);
          if (t + KDEPTH < 4) {
#pragma unroll
            for (int r = 0; r < 8; ++r) glds16s(kvp + (size_t)(32 * (t + KDEPTH) + r) * 512, (u32)((lane ^ swz(rb + r)) * 16), ldst + (32 * (t + KDEPTH) + r) * 1024);
          }
        }
        if (t == 3) {
#pragma unroll
          for (int u = 0; u < 8; ++u) qv[u] = __builtin_amdgcn_raw_buffer_load_b128(rs, WS_QA + (ab * 4096 + tid) * 16, u * 8192, 16);
        }
        lds_wait(L, C_KQ + t, 4);
        if (t == 3) STAMP(2);
        const int k0 = 32 * t + 8 * gq + q4, k1 = k0 + 4;
#pragma unroll
        for (int tt = 0; tt < 4; ++tt) {
          const v2u lo = trread(L + L_BIG + k0 * 1024 + (((8 * wave + 2 * tt + (p4 >> 1)) ^ swz(k0)) * 16) + 8 * (p4 & 1));
          const v2u hi = trread(L + L_BIG + k1 * 1024 + (((8 * wave + 2 * tt + (p4 >> 1)) ^ swz(k1)) * 16) + 8 * (p4 & 1));
          vf[t][tt] = v4u{lo.x, lo.y, hi.x, hi.y};
        }
        if ((wave >> 1) == t) {
          LAS unsigned char* kvrow = L + L_BIG + (16 * wave + i16) * 1024;
#pragma unroll
          for (int u = 0; u < 16; ++u) sf[u] = *(const LAS v4u*)(kvrow + 256 * (u >> 2) + 16 * ((4 * (u & 3) + gq) ^ sz));
        }
        if (t == 0) lds_signal(L, C_PR, lane);
      }
    }
    STAMP(3);
    {
      const u32 src = WS_QA + (ab * 4096 + tid) * 16;
      v4u* v = qv;
      _Pragma("unroll 1") for (u32 spins = 0; spins < SPIN_CAP; ++spins) {
        u32 bad = 0u;
#pragma unroll
        for (int u = 0; u < 8; ++u) bad |= BADTAG(v[u]);
        if (__all(bad == 0u)) break;
        __builtin_amdgcn_s_sleep(2);
        CFENCE();
#pragma unroll
        for (int u = 0; u < 8; ++u) if (BADTAG(v[u]) != 0u) v[u] = __builtin_amdgcn_raw_buffer_load_b128(rs, src, u * 8192, 16);
      }
      const int cu = tid & 127, hb = tid >> 7;
      lds_wait(L, C_PR, 8);
#pragma unroll
      for (int u = 0; u < 8; ++u) {
        const int hh = 4 * u + hb;
        *(LAS v2u*)(L + L_BIG + hh * 1024 + (((cu >> 1) ^ swz(hh)) * 16) + (cu & 1) * 8) = v2u{v[u].x, v[u].z};
      }
    }
    STAMP(4);
    BAR();
    STAMP(5);
    f4 s0 = {0.f, 0.f, 0.f, 0.f}, s1 = s0;
    {
      LAS unsigned char* qrow = L + L_BIG + i16 * 1024;
      v4u qa[4], qb[4];
#pragma unroll
      for (int t = 0; t < 4; ++t) {
        const u32 off = 16 * ((4 * t + gq) ^ sz);
        qa[t] = *(const LAS v4u*)(qrow + off); qb[t] = *(const LAS v4u*)(qrow + 16384 + off);
      }
      __builtin_amdgcn_sched_barrier(0);
#pragma unroll
      for (int t = 0; t < 16; ++t) {
        s0 = mfma16(sf[t], qa[t & 3], s0);
        s1 = mfma16(sf[t], qb[t & 3], s1);
        if (t < 4)
          __builtin_amdgcn_raw_buffer_store_b128(sv, rs, WS_IN3 + ((((8 * t + (lane >> 3)) * 4 + (wave >> 1)) * 4 + ab) * 32 + as) * 256 + (wave & 1) * 128 + (lane & 7) * 16, 0, 16);
        if (t + 4 < 16) {
          const u32 off = 256 * ((t + 4) >> 2) + 16 * ((4 * (t & 3) + gq) ^ sz);
          qa[t & 3] = *(const LAS v4u*)(qrow + off); qb[t & 3] = *(const LAS v4u*)(qrow + 16384 + off);
        }
        __builtin_amdgcn_sched_barrier(0);
      }
    }
    {
      float mx0 = maxx32(maxx16(fmaxf(fmaxf(s0[0], s0[1]), fmaxf(s0[2], s0[3]))));
      float mx1 = maxx32(maxx16(fmaxf(fmaxf(s1[0], s1[1]), fmaxf(s1[2], s1[3]))));
      if (lane < 32) *(LAS float*)(L + M_WMAX + lane * 48 + wave * 4) = lane < 16 ? mx0 : mx1;
    }
    STAMP(6);
    BAR();
    float m0, m1;
    {
      const f4 a0 = *(const LAS f4*)(L + M_WMAX + i16 * 48), a1 = *(const LAS f4*)(L + M_WMAX + i16 * 48 + 16);
      const f4 b0 = *(const LAS f4*)(L + M_WMAX + 768 + i16 * 48), b1 = *(const LAS f4*)(L + M_WMAX + 768 + i16 * 48 + 16);
      m0 = fmaxf(fmaxf(fmaxf(a0[0], a0[1]), fmaxf(a0[2], a0[3])), fmaxf(fmaxf(a1[0], a1[1]), fmaxf(a1[2], a1[3])));
      m1 = fmaxf(fmaxf(fmaxf(b0[0], b0[1]), fmaxf(b0[2], b0[3])), fmaxf(fmaxf(b1[0], b1[1]), fmaxf(b1[2], b1[3])));
      const u32 pa = pk_bf16(__builtin_amdgcn_exp2f(s0[0] - m0), __builtin_amdgcn_exp2f(s0[1] - m0));
      const u32 pb = pk_bf16(__builtin_amdgcn_exp2f(s0[2] - m0), __builtin_amdgcn_exp2f(s0[3] - m0));
      const u32 pc = pk_bf16(__builtin_amdgcn_exp2f(s1[0] - m1), __builtin_amdgcn_exp2f(s1[1] - m1));
      const u32 pd = pk_bf16(__builtin_amdgcn_exp2f(s1[2] - m1), __builtin_amdgcn_exp2f(s1[3] - m1));
      const u32 poff = i16 * 256 + (((2 * wave + (gq >> 1)) ^ sz) * 16) + (gq & 1) * 8;
      *(LAS v2u*)(L + L_P + poff) = v2u{pa, pb};
      *(LAS v2u*)(L + L_P + 4096 + poff) = v2u{pc, pd};
      const float l0 = sumx32(sumx16((lo_f(pa) + hi_f(pa)) + (lo_f(pb) + hi_f(pb))));
      const float l1 = sumx32(sumx16((lo_f(pc) + hi_f(pc)) + (lo_f(pd) + hi_f(pd))));
      if (lane < 32) *(LAS float*)(L + M_WSUM + lane * 48 + wave * 4) = lane < 16 ? l0 : l1;
    }
    VMWAIT(0);
    BAR();
    STAMP(7);
    if (wave == 0 && lane < 32) {
      const f4 la = *(const LAS f4*)(L + M_WSUM + lane * 48), lb = *(const LAS f4*)(L + M_WSUM + lane * 48 + 16);
      const float l = ((la[0] + la[1]) + (la[2] + la[3])) + ((lb[0] + lb[1]) + (lb[2] + lb[3]));
      const u32 lse = __float_as_uint((lane < 16 ? m0 : m1) + __builtin_amdgcn_logf(l));
      __builtin_amdgcn_raw_buffer_store_b128(v4u{lse, tagA, lse, tagB}, rs, WS_ST3 + ((lane * 4 + ab) * 32 + as) * 16, 0, 16);
    }
    v4u fu = {0u, 0u, 0u, 0u}, wd0 = fu, wd1 = fu;
    if (do_ffn) {
      fu = __builtin_amdgcn_raw_buffer_load_b128(rs, WS_FX + fg * 8192 + tid * 16, 0, 16);
      wd0 = ldnt(wd + (size_t)(16 * fg + 2 * (lane >> 3)) * 512 + 64 * wave + 8 * (lane & 7));
      wd1 = ldnt(wd + (size_t)(16 * fg + 2 * (lane >> 3) + 1) * 512 + 64 * wave + 8 * (lane & 7));
    }
    const u16 xfin = x[ab * 512 + as * 16 + (tid & 15)];
    __builtin_amdgcn_sched_barrier(0);
#pragma unroll
    for (int ht = 0; ht < 2; ++ht) {
      f4 oacc[4];
#pragma unroll
      for (int tt = 0; tt < 4; ++tt) oacc[tt] = f4{0.f, 0.f, 0.f, 0.f};
#pragma unroll
      for (int t = 0; t < 4; ++t) {
        const v4u pf = *(const LAS v4u*)(L + L_P + ht * 4096 + i16 * 256 + (((4 * t + gq) ^ sz) * 16));
#pragma unroll
        for (int tt = 0; tt < 4; ++tt) oacc[tt] = mfma16(vf[t][tt], pf, oacc[tt]);
      }
      const f4 la = *(const LAS f4*)(L + M_WSUM + ht * 768 + i16 * 48), lb = *(const LAS f4*)(L + M_WSUM + ht * 768 + i16 * 48 + 16);
      const float l = ((la[0] + la[1]) + (la[2] + la[3])) + ((lb[0] + lb[1]) + (lb[2] + lb[3]));
      const float il = __builtin_amdgcn_rcpf(l);
      const int head = 16 * ht + i16;
      LAS unsigned char* priv = L + L_BIG + 65536 + wave * 4608;
#pragma unroll
      for (int tt = 0; tt < 4; ++tt)
        *(LAS v2u*)(priv + head * 144 + tt * 32 + gq * 8) = v2u{pk_bf16(oacc[tt][0] * il, oacc[tt][1] * il), pk_bf16(oacc[tt][2] * il, oacc[tt][3] * il)};
#pragma unroll
      for (int e = 0; e < 2; ++e) {
        const int hd = 16 * ht + 8 * e + (lane >> 3);
        const v4u o = *(const LAS v4u*)(priv + hd * 144 + (lane & 7) * 16);
        __builtin_amdgcn_raw_buffer_store_b128(nosent(o), rs,
            WS_IN3 + (((hd * 4 + (wave >> 1)) * 4 + ab) * 32 + as) * 256 + (wave & 1) * 128 + (lane & 7) * 16, 0, 16);
      }
    }
    STAMP(8);
    if (do_ffn) {
      const int chunk = lane & 7, rg = lane >> 3;
      _Pragma("unroll 1") for (u32 spins = 0; spins < SPIN_CAP; ++spins) {
        if (__all(((fu.y ^ tagA) | (fu.w ^ tagB)) == 0u)) break;
        CFENCE();
        fu = __builtin_amdgcn_raw_buffer_load_b128(rs, WS_FX + fg * 8192 + tid * 16, 0, 16);
      }
      *(LAS v2u*)(L + L_BIG + 32768 + tid * 8) = v2u{fu.x, fu.z};
      BAR();
      if (lane < 32) {
        float g0 = 0.f, g1 = 0.f, u0 = 0.f, u1 = 0.f;
#pragma unroll
        for (int w = 0; w < 8; ++w) {
          const v2u gv = *(const LAS v2u*)(L + L_BIG + 32768 + (w * 64 + lane) * 8), uv = *(const LAS v2u*)(L + L_BIG + 32768 + (w * 64 + 32 + lane) * 8);
          g0 += __uint_as_float(gv.x); g1 += __uint_as_float(gv.y); u0 += __uint_as_float(uv.x); u1 += __uint_as_float(uv.y);
        }
        g0 = rbf(g0); g1 = rbf(g1);
        const float h0 = rbf(rbf(g0 / (1.0f + __expf(-g0))) * rbf(u0)), h1 = rbf(rbf(g1 / (1.0f + __expf(-g1))) * rbf(u1));
        *(LAS v2u*)(L + L_BIG + 40960 + wave * 256 + lane * 8) = v2u{__float_as_uint(h0), __float_as_uint(h1)};
      }
      float y[32];
#pragma unroll
      for (int b = 0; b < 4; ++b) {
        const v2u hp = *(const LAS v2u*)(L + L_BIG + 40960 + wave * 256 + (b * 16 + 2 * rg) * 4);
        const float h0 = __uint_as_float(hp.x), h1 = __uint_as_float(hp.y);
#pragma unroll
        for (int e = 0; e < 4; ++e) {
          y[b * 8 + 2 * e] = h0 * lo_f(wd0[e]) + h1 * lo_f(wd1[e]);
          y[b * 8 + 2 * e + 1] = h0 * hi_f(wd0[e]) + h1 * hi_f(wd1[e]);
        }
      }
      float n16[16], n8[8];
#pragma unroll
      for (int k = 0; k < 16; ++k) n16[k] = tred32(y[k], y[16 + k]);
#pragma unroll
      for (int k = 0; k < 8; ++k) { n8[k] = tred16(n16[k], n16[8 + k]); n8[k] += dpp<0x128>(n8[k]); }
      LAS unsigned char* priv = L + L_BIG + 65536 + wave * 4608;
      const f4 oh = (lane & 8) ? f4{n8[4], n8[5], n8[6], n8[7]} : f4{n8[0], n8[1], n8[2], n8[3]};
      *(LAS f4*)(priv + (gq * 16 + chunk * 2 + ((lane >> 3) & 1)) * 16) = oh;
      const v4u o = *(const LAS v4u*)(priv + lane * 16);
      const int f = gq * 32 + 4 * wave + (i16 >> 2);
      __builtin_amdgcn_raw_buffer_store_b128(nosent(o), rs, WS_INY + (f * 112 + fg) * 64 + (i16 & 3) * 16, 0, 16);
    }
    STAMP(9);
    float gate = 0.f;
    {
      const u32 sy = WS_INY + kb * 7168 + (tid < 448 ? tid : 0) * 16;
      v4u y0 = sv;
      __builtin_amdgcn_s_sleep(8);
      _Pragma("unroll 1") for (u32 spins = 0; spins < SPIN_CAP; ++spins) {
        CFENCE();
        if (umax4(y0) == SENT) y0 = __builtin_amdgcn_raw_buffer_load_b128(rs, sy, 0, 16);
        if (__all(umax4(y0) != SENT)) break;
        __builtin_amdgcn_s_sleep(4);
      }
      float v[4], n2[2];
#pragma unroll
      for (int e = 0; e < 4; ++e) v[e] = tid < 448 ? __uint_as_float(y0[e]) : 0.f;
#pragma unroll
      for (int e = 0; e < 2; ++e) n2[e] = tred32(v[e], v[2 + e]);
      float n1 = tred16(n2[0], n2[1]);
      n1 += dpp<0x124>(n1); n1 += dpp<0x128>(n1);
      if (i16 < 4) *(LAS float*)(L + M_FIN + (wave * 16 + i16 * 4 + gq) * 4) = n1;
      BAR();
      if (tid < 16) {
        const int m = as * 16 + tid;
        float y = 0.f;
#pragma unroll
        for (int w = 0; w < 8; ++w) y += *(const LAS float*)(L + M_FIN + (w * 16 + tid) * 4);
        const float rsv = *(const LAS float*)(L + M_RS + ab * 4);
        const float xnv = bf2f(*(const LAS u16*)(L + L_XN + ab * XS + m * 2));
        y = rbf(rbf(y) * rsv);
        const float z = rbf(xnv + y);
        gate = rbf(z / (1.0f + __expf(-z)));
      }
    }
    STAMP(13);
    {
      const u32 s5 = WS_IN5 + kb * 8192 + tid * 16;
      v4u a0 = sv;
      _Pragma("unroll 1") for (u32 spins = 0; spins < SPIN_CAP; ++spins) {
        CFENCE();
        if (umax4(a0) == SENT) a0 = __builtin_amdgcn_raw_buffer_load_b128(rs, s5, 0, 16);
        if (__all(umax4(a0) != SENT)) break;
        __builtin_amdgcn_s_sleep(1);
      }
      STAMP(10);
      float n2[2];
#pragma unroll
      for (int e = 0; e < 2; ++e) n2[e] = tred32(__uint_as_float(a0[e]), __uint_as_float(a0[2 + e]));
      float n1 = tred16(n2[0], n2[1]);
      n1 += dpp<0x124>(n1); n1 += dpp<0x128>(n1);
      if (i16 < 4) *(LAS float*)(L + M_FIN + 512 + (wave * 16 + i16 * 4 + gq) * 4) = n1;
    }
    BAR();
    if (tid < 16) {
      float sacc = 0.f;
#pragma unroll
      for (int w = 0; w < 8; ++w) sacc += *(const LAS float*)(L + M_FIN + 512 + (w * 16 + tid) * 4);
      const float o = bf2f(xfin) + rbf(rbf(sacc) * gate);
      out[ab * 512 + as * 16 + tid] = __builtin_bit_cast(u16, (__bf16)o);
    }
    if (g == 0 && tid == 0) __builtin_amdgcn_raw_buffer_store_b64(v2u{tagA, tagB}, rs, WS_EPOCH, 0, 16);
  }
}

extern "C" void kernel_launch(void* const* d_in, const int* in_sizes, int n_in,
                              void* d_out, int out_size, void* d_ws, size_t ws_size,
                              hipStream_t stream) {
  (void)in_sizes; (void)n_in; (void)out_size;
  (void)hipFuncSetAttribute((const void*)mega, hipFuncAttributeMaxDynamicSharedMemorySize, LDS_BYTES);
  u64* st = (u64*)((char*)d_ws + ws_size - (1 << 20));
  mega<<<NWG, 512, LDS_BYTES, stream>>>((const u16*)d_in[0], (const u16*)d_in[1], (const u16*)d_in[2], (const u16*)d_in[3],
                                        (const u16*)d_in[4], (const u16*)d_in[5], (const u16*)d_in[7], (const u16*)d_in[8],
                                        (const u16*)d_in[9], (const u16*)d_in[10], (u16*)d_out, (unsigned char*)d_ws, st);
}
```

```cpp
#include <hip/hip_runtime.h>
#include <stdint.h>

typedef unsigned short u16;
typedef unsigned int u32;
typedef unsigned long long u64;
typedef u32 v4u __attribute__((ext_vector_type(4)));
typedef u32 v2u __attribute__((ext_vector_type(2)));
typedef float f4 __attribute__((ext_vector_type(4)));
typedef short bf16x8 __attribute__((ext_vector_type(8)));
typedef short s16x4 __attribute__((ext_vector_type(4)));
typedef __bf16 bf16x2 __attribute__((ext_vector_type(2)));
#define LAS __attribute__((address_space(3)))
#define GAS __attribute__((address_space(1)))

#define STAMPS 0

constexpr int KDEPTH = 3;
constexpr int CPF_KB = 20;
constexpr int NWG = 256;
constexpr u32 WS_EPOCH = 0;
constexpr u32 WS_QX = 4096;
constexpr u32 WS_FX = WS_QX + 32 * 8192;
constexpr u32 WS_QA = WS_FX + 112 * 8192;
constexpr u32 WS_IN3 = WS_QA + 4 * 32 * 128 * 16;
constexpr u32 WS_ST3 = WS_IN3 + 32 * 4 * 32768;
constexpr u32 WS_IN5 = WS_ST3 + 32 * 2048;
constexpr u32 WS_INY = WS_IN5 + 128 * 8192;
constexpr u32 WS_END = WS_INY + 128 * 7168;
constexpr int L_BIG = 0;
constexpr int L_XN = 131072;
constexpr int XS = 1088;
constexpr int L_P = L_XN + 4352;
constexpr int L_M = L_P + 8192;
constexpr int M_RED = L_P;
constexpr int M_QS = L_M;
constexpr int M_LSE = L_M + 1280;
constexpr int M_WMAX = L_M + 2048;
constexpr int M_WSUM = L_M + 3584;
constexpr int M_OCS = L_M + 4096;
constexpr int M_CTX = L_M + 5120;
constexpr int M_OC8 = L_M + 2048;
constexpr int M_CX8 = M_OC8 + 8 * 272;
constexpr int M_H = L_M + 6144;
constexpr int M_FIN = L_M + 6400;
constexpr int M_RS = L_M + 7424;
constexpr int M_CNT = L_M + 7488;
constexpr int L_DUMMY = L_M + 7680;
constexpr int LDS_BYTES = L_DUMMY + 1024;
enum { C_XN = 0, C_T = 1, C_RED = 2, C_CLR = 3, C_KV = 4, C_A = 5, C_UV = 6, C_UVR = 7, C_F = 8, C_FR = 9, C_H = 10, C_T2 = 11, C_QS = 12, C_LSE = 13, C_FA = 14, C_PR = 15, C_KQ = 16  , C_WO = 20 };
constexpr u32 SENT = 0xFFFFFFFFu;
constexpr u32 SPIN_CAP = 1u << 16;

__device__ __forceinline__ float bf2f(u32 v) { return __uint_as_float(v << 16); }
__device__ __forceinline__ float lo_f(u32 v) { return __uint_as_float(v << 16); }
__device__ __forceinline__ float hi_f(u32 v) { return __uint_as_float(v & 0xffff0000u); }
__device__ __forceinline__ u32 pk_bf16(float a, float b) {
  bf16x2 r; r[0] = (__bf16)a; r[1] = (__bf16)b; return __builtin_bit_cast(u32, r);
}
__device__ __forceinline__ float rbf(float a) { return (float)((__bf16)a); }
__device__ __forceinline__ f4 mfma16(v4u a, v4u b, f4 c) {
  return __builtin_amdgcn_mfma_f32_16x16x32_bf16(__builtin_bit_cast(bf16x8, a), __builtin_bit_cast(bf16x8, b), c, 0, 0, 0);
}
__device__ __forceinline__ float dot2(u32 a, u32 b, float c) {
  return __builtin_amdgcn_fdot2_f32_bf16(__builtin_bit_cast(bf16x2, a), __builtin_bit_cast(bf16x2, b), c, false);
}
__device__ __forceinline__ u32 swz(u32 row) { return ((((row >> 3) ^ (row >> 2)) & 1u) << 3) | ((row & 3u) << 1) | ((row >> 3) & 1u); }
__device__ __forceinline__ v2u trread(LAS unsigned char* p) {
  return __builtin_bit_cast(v2u, __builtin_amdgcn_ds_read_tr16_b64_v4i16((LAS s16x4*)p));
}
__device__ __forceinline__ void glds16s(const void* sbase, u32 voff, u32 lds_dst) {
  u32 keep;
  asm volatile("s_mov_b32 %0, m0\n\ts_mov_b32 m0, %3\n\ts_nop 0\n\tglobal_load_lds_dwordx4 %1, %2 nt\n\ts_mov_b32 m0, %0"
               : "=&s"(keep) : "v"(voff), "s"(sbase), "s"(lds_dst) : "memory");
}
__device__ __forceinline__ void glds16v(const void* gsrc, u32 lds_dst) {
  u32 keep;
  asm volatile("s_mov_b32 %0, m0\n\ts_mov_b32 m0, %2\n\ts_nop 0\n\tglobal_load_lds_dwordx4 %1, off nt\n\ts_mov_b32 m0, %0"
               : "=&s"(keep) : "v"(gsrc), "s"(lds_dst) : "memory");
}
__device__ __forceinline__ v4u ldnt(const void* p) { return __builtin_nontemporal_load((const GAS v4u*)p); }
template <int CTRL> __device__ __forceinline__ float dpp(float v) {
  return __int_as_float(__builtin_amdgcn_update_dpp(0, __float_as_int(v), CTRL, 0xf, 0xf, true));
}
__device__ __forceinline__ float sum16(float v) { v += dpp<0xB1>(v); v += dpp<0x4E>(v); v += dpp<0x141>(v); v += dpp<0x140>(v); return v; }
__device__ __forceinline__ float sumx16(float v) { auto r = __builtin_amdgcn_permlane16_swap(__float_as_uint(v), __float_as_uint(v), false, false); return __uint_as_float(r[0]) + __uint_as_float(r[1]); }
__device__ __forceinline__ float sumx32(float v) { auto r = __builtin_amdgcn_permlane32_swap(__float_as_uint(v), __float_as_uint(v), false, false); return __uint_as_float(r[0]) + __uint_as_float(r[1]); }
__device__ __forceinline__ float maxx16(float v) { auto r = __builtin_amdgcn_permlane16_swap(__float_as_uint(v), __float_as_uint(v), false, false); return fmaxf(__uint_as_float(r[0]), __uint_as_float(r[1])); }
__device__ __forceinline__ float maxx32(float v) { auto r = __builtin_amdgcn_permlane32_swap(__float_as_uint(v), __float_as_uint(v), false, false); return fmaxf(__uint_as_float(r[0]), __uint_as_float(r[1])); }
__device__ __forceinline__ float sum64(float v) { return sumx32(sumx16(sum16(v))); }
__device__ __forceinline__ float tred32(float a, float b) { auto r = __builtin_amdgcn_permlane32_swap(__float_as_uint(a), __float_as_uint(b), false, false); return __uint_as_float(r[0]) + __uint_as_float(r[1]); }
__device__ __forceinline__ float tred16(float a, float b) { auto r = __builtin_amdgcn_permlane16_swap(__float_as_uint(a), __float_as_uint(b), false, false); return __uint_as_float(r[0]) + __uint_as_float(r[1]); }

#define BAR() do { asm volatile("s_waitcnt lgkmcnt(0)" ::: "memory"); __builtin_amdgcn_s_barrier(); asm volatile("" ::: "memory"); } while (0)
#define BADTAG(v) (((v).y ^ tagA) | ((v).w ^ tagB))
#define CFENCE() asm volatile("" ::: "memory")
#define VMWAIT(n) asm volatile("s_waitcnt vmcnt(" #n ")" ::: "memory")
__device__ __forceinline__ u32 umax4(v4u v) { u32 a = v.x > v.y ? v.x : v.y, b = v.z > v.w ? v.z : v.w; return a > b ? a : b; }
__device__ __forceinline__ v4u nosent(v4u v) {
  v.x = v.x < 0xFFFFFFFEu ? v.x : 0xFFFFFFFEu; v.y = v.y < 0xFFFFFFFEu ? v.y : 0xFFFFFFFEu;
  v.z = v.z < 0xFFFFFFFEu ? v.z : 0xFFFFFFFEu; v.w = v.w < 0xFFFFFFFEu ? v.w : 0xFFFFFFFEu; return v;
}
__device__ __forceinline__ void lds_signal(LAS unsigned char* L, int c, int lane) {
  asm volatile("s_waitcnt lgkmcnt(0)" ::: "memory");
  if (lane == 0) __hip_atomic_fetch_add((LAS u32*)(L + M_CNT) + c, 1u, __ATOMIC_RELAXED, __HIP_MEMORY_SCOPE_WORKGROUP);
}
__device__ __forceinline__ void lds_wait(LAS unsigned char* L, int c, u32 target) {
  _Pragma("unroll 1") for (u32 spins = 0; spins < (1u << 22); ++spins) {
    if (*((volatile LAS u32*)(L + M_CNT) + c) >= target) break;
    __builtin_amdgcn_s_sleep(0);
  }
  CFENCE();
}

__device__ __forceinline__ void ln_row(LAS unsigned char* L, v4u xv, v4u wrv, int r, int lane) {
  float xf[8] = {lo_f(xv.x), hi_f(xv.x), lo_f(xv.y), hi_f(xv.y), lo_f(xv.z), hi_f(xv.z), lo_f(xv.w), hi_f(xv.w)};
  float s = ((xf[0] + xf[1]) + (xf[2] + xf[3])) + ((xf[4] + xf[5]) + (xf[6] + xf[7]));
  const float mean = sum64(s) * (1.0f / 512.0f);
  float q2 = 0.f;
#pragma unroll
  for (int e = 0; e < 8; ++e) { const float d = xf[e] - mean; q2 += d * d; }
  const float var = rbf(sum64(q2) * (1.0f / 512.0f));
  const float mb = rbf(mean);
  const float den = rbf(sqrtf(rbf(var + 1e-5f)));
  float xn[8];
#pragma unroll
  for (int e = 0; e < 8; ++e) xn[e] = rbf(xf[e] - mb) / den;
  v4u xnp = {pk_bf16(xn[0], xn[1]), pk_bf16(xn[2], xn[3]), pk_bf16(xn[4], xn[5]), pk_bf16(xn[6], xn[7])};
  *(LAS v4u*)(L + L_XN + r * XS + lane * 16) = xnp;
  lds_signal(L, C_XN, lane);
  float rr = dot2(xnp.x, wrv.x, 0.f); rr = dot2(xnp.y, wrv.y, rr); rr = dot2(xnp.z, wrv.z, rr); rr = dot2(xnp.w, wrv.w, rr);
  rr = rbf(sum64(rr));
  if (lane == 0) *(LAS float*)(L + M_RS + r * 4) = rbf(1.0f / (1.0f + __expf(-rr)));
}

__device__ __forceinline__ u32 gsw(u32 row) { return (((row >> 1) & 1u) | (((row >> 3) & 1u) << 1)) << 1; }
__device__ __forceinline__ void tile_gemv(LAS unsigned char* L, int tile_off, int m0, int kq, int lane) {
  const int i16 = lane & 15, gq = lane >> 4, q4 = i16 >> 2, p4 = i16 & 3;
  f4 acc[4];
#pragma unroll
  for (int dt = 0; dt < 4; ++dt) acc[dt] = f4{0.f, 0.f, 0.f, 0.f};
  const u32 fl = gsw(8 * gq + q4);
#pragma unroll
  for (int t = 0; t < 2; ++t) {
    const int r0 = 64 * kq + 32 * t + 8 * gq + q4;
    const v4u xf = *(const LAS v4u*)(L + L_XN + (i16 & 3) * XS + (m0 + 64 * kq + 32 * t + 8 * gq) * 2);
    LAS unsigned char* b0 = L + tile_off + r0 * 128 + 8 * (p4 & 1);
#pragma unroll
    for (int dt = 0; dt < 4; ++dt) {
      const v2u a0 = trread(b0 + (((2 * dt + (p4 >> 1)) ^ fl) * 16)), a1 = trread(b0 + 512 + (((2 * dt + (p4 >> 1)) ^ fl) * 16));
      acc[dt] = mfma16(v4u{a0.x, a0.y, a1.x, a1.y}, xf, acc[dt]);
    }
  }
  if (i16 < 4) {
#pragma unroll
    for (int dt = 0; dt < 4; ++dt) *(LAS f4*)(L + M_RED + (((kq * 4 + dt) * 4 + gq) * 4 + i16) * 16) = acc[dt];
  }
}

__global__ __launch_bounds__(512, 2) void mega(const u16* __restrict__ x, const u16* __restrict__ ckv, const u16* __restrict__ wq,
                                               const u16* __restrict__ wuk, const u16* __restrict__ wuv, const u16* __restrict__ wo,
                                               const u16* __restrict__ wr, const u16* __restrict__ wg, const u16* __restrict__ wu,
                                               const u16* __restrict__ wd, u16* __restrict__ out, unsigned char* ws, u64* stamps) {
  extern __shared__ __attribute__((aligned(16))) unsigned char lds_raw[];
  LAS unsigned char* L = (LAS unsigned char*)lds_raw;
  const int tid = threadIdx.x, lane = tid & 63;
  const int wave = __builtin_amdgcn_readfirstlane(tid >> 6);
  const int g = blockIdx.x, xg = g & 7, idx = g >> 3;
  const int i16 = lane & 15, gq = lane >> 4, q4 = i16 >> 2, p4 = i16 & 3;
  const __amdgpu_buffer_rsrc_t rs = __builtin_amdgcn_make_buffer_rsrc((void*)ws, 0, (int)WS_END, 0x00020000);
  const u32 lds_base = (u32)(size_t)lds_raw;
  const v4u sv = {SENT, SENT, SENT, SENT};
#define STAMP(k)
  asm volatile("" :: "s"(x), "s"(ckv), "s"(wq), "s"(wuk), "s"(wuv), "s"(wo), "s"(wr), "s"(wg), "s"(wu), "s"(wd), "s"(out), "s"(ws));
  {
    u64 pc;
    asm volatile("s_getpc_b64 %0" : "=s"(pc));
    const int slice = idx;
    if (wave == 3 && slice < CPF_KB) {
      u32 keep;
      asm volatile("s_mov_b32 %0, m0\n\ts_mov_b32 m0, %3\n\ts_nop 0\n\tglobal_load_lds_dwordx4 %1, %2\n\ts_mov_b32 m0, %0"
                   : "=&s"(keep) : "v"((u32)(slice * 1024 + lane * 16)), "s"(pc & ~(u64)63), "s"(lds_base + L_DUMMY) : "memory");
    }
  }
  STAMP(0);
  if (tid < 24) *((LAS u32*)(L + M_CNT) + tid) = 0u;
  u32 tagA = 0u, tagB = 0u;
#define READ_EPOCH() do { \
    v2u ep_; \
    asm volatile("s_load_dwordx2 %0, %1, 0x0\n\ts_waitcnt lgkmcnt(0)" : "=s"(ep_) : "s"(ws) : "memory"); \
    tagA = ep_.x + 1u; tagB = ep_.y + 1u; } while (0)
#define nv (v4u{0u, tagA ^ 1u, 0u, 0u})
  if (idx >= 16) {
    const int wi = idx - 16, h = xg * 4 + (wi >> 2), j = wi & 3, wid = xg * 16 + wi;
    if (wave < 4) {
      const int a = wave;
      const v4u xv = *(const GAS v4u*)(x + a * 512 + lane * 8);
      const v4u wrv = *(const GAS v4u*)(wr + lane * 8);
      BAR();
      ln_row(L, xv, wrv, a, lane);
      READ_EPOCH();
      if (lane < 32) {
        __builtin_amdgcn_raw_buffer_store_b128(nv, rs, WS_QX + (((h * 4 + a) * 2 + (j >> 1)) * 64 + 32 * (j & 1) + lane) * 16, 0, 16);
        __builtin_amdgcn_raw_buffer_store_b128(nv, rs, WS_QA + (((lane >> 3) * 32 + h) * 128 + 32 * j + 8 * a + (lane & 7)) * 16, 0, 16);
      }
      lds_wait(L, C_XN, 4);
      lds_wait(L, C_T, 4);
      STAMP(1);
      tile_gemv(L, L_BIG, 256 * (j >> 1), a, lane);
      lds_signal(L, C_RED, lane);
      lds_wait(L, C_RED, 4);
      if (lane < 32) {
        float s0 = 0.f, s1 = 0.f;
#pragma unroll
        for (int kq = 0; kq < 4; ++kq) {
          const v2u pv = *(const LAS v2u*)(L + M_RED + ((kq * 16 + (lane >> 1)) * 4 + a) * 16 + (lane & 1) * 8);
          s0 += __uint_as_float(pv.x); s1 += __uint_as_float(pv.y);
        }
        __builtin_amdgcn_raw_buffer_store_b128(v4u{__float_as_uint(s0), tagA, __float_as_uint(s1), tagB}, rs,
                                               WS_QX + (((h * 4 + a) * 2 + (j >> 1)) * 64 + 32 * (j & 1) + lane) * 16, 0, 16);
      }
      STAMP(2);
      lds_wait(L, C_T2, 4);
      v4u wukf[2][4];
#pragma unroll
      for (int ct = 0; ct < 2; ++ct)
#pragma unroll
        for (int t = 0; t < 4; ++t)
          wukf[ct][t] = *(const LAS v4u*)(L + L_BIG + 32768 + (32 * a + 16 * ct + i16) * 256 + (((4 * t + gq) ^ swz(i16)) * 16));
      v4u qu[2];
      qu[0] = qu[1] = v4u{0u, tagA ^ 1u, 0u, 0u};
      const u32 qsrc = WS_QX + ((h * 4 + a) * 128 + lane) * 16;
      _Pragma("unroll 1") for (u32 spins = 0; spins < SPIN_CAP; ++spins) {
        CFENCE();
#pragma unroll
        for (int u = 0; u < 2; ++u)
          if (((qu[u].y ^ tagA) | (qu[u].w ^ tagB)) != 0u) qu[u] = __builtin_amdgcn_raw_buffer_load_b128(rs, qsrc + u * 1024, 0, 16);
        const u32 bad = (qu[0].y ^ tagA) | (qu[0].w ^ tagB) | (qu[1].y ^ tagA) | (qu[1].w ^ tagB);
        if (__all(bad == 0u)) break;
      }
      *(LAS u32*)(L + M_QS + a * 320 + lane * 4) = pk_bf16(__uint_as_float(qu[0].x) + __uint_as_float(qu[1].x), __uint_as_float(qu[0].z) + __uint_as_float(qu[1].z));
      lds_signal(L, C_QS, lane);
      lds_wait(L, C_QS, 4);
      STAMP(3);
      LAS unsigned char* priv = L + L_BIG + 65536 + a * 2048;
      f4 qa0 = {0.f, 0.f, 0.f, 0.f}, qa1 = qa0;
#pragma unroll
      for (int t = 0; t < 4; ++t) {
        const v4u qf = *(const LAS v4u*)(L + M_QS + (i16 & 3) * 320 + (32 * t + 8 * gq) * 2);
        qa0 = mfma16(wukf[0][t], qf, qa0);
        qa1 = mfma16(wukf[1][t], qf, qa1);
      }
      const float SC = 0.08838834764831845f * 1.4426950408889634f;
      if (i16 < 4) {
        *(LAS v2u*)(priv + 1024 + (i16 * 8 + gq) * 8) = v2u{pk_bf16(qa0[0] * SC, qa0[1] * SC), pk_bf16(qa0[2] * SC, qa0[3] * SC)};
        *(LAS v2u*)(priv + 1024 + (i16 * 8 + 4 + gq) * 8) = v2u{pk_bf16(qa1[0] * SC, qa1[1] * SC), pk_bf16(qa1[2] * SC, qa1[3] * SC)};
      }
      lds_wait(L, C_CLR, 4);
      if (lane < 32) {
        const v2u pv = *(const LAS v2u*)(priv + 1024 + lane * 8);
        __builtin_amdgcn_raw_buffer_store_b128(v4u{pv.x, tagA, pv.y, tagB}, rs, WS_QA + (((lane >> 3) * 32 + h) * 128 + 32 * j + 8 * a + (lane & 7)) * 16, 0, 16);
      }
      lds_signal(L, C_A, lane);
      STAMP(4);
      if (wi < 14) {
        v4u fw[8];
        const int mat = wave & 1, ks = wave >> 1;
        const u16* src = (mat ? wu : wg) + (size_t)(64 * xg + 32 * ks + 8 * gq) * 1792 + 128 * wi + 8 * i16;
#pragma unroll
        for (int jj = 0; jj < 8; ++jj) fw[jj] = ldnt(src + (size_t)jj * 1792);
        const v4u af = *(const LAS v4u*)(L + L_XN + (i16 >> 2) * XS + (64 * xg + 32 * ks + 8 * gq) * 2);
        float pr[8];
#pragma unroll
        for (int cc = 0; cc < 8; ++cc) {
          const u32 sel = (cc & 1) ? 0x07060302u : 0x05040100u;
          v4u bfr;
          bfr.x = __builtin_amdgcn_perm(fw[1][cc >> 1], fw[0][cc >> 1], sel);
          bfr.y = __builtin_amdgcn_perm(fw[3][cc >> 1], fw[2][cc >> 1], sel);
          bfr.z = __builtin_amdgcn_perm(fw[5][cc >> 1], fw[4][cc >> 1], sel);
          bfr.w = __builtin_amdgcn_perm(fw[7][cc >> 1], fw[6][cc >> 1], sel);
          pr[cc] = mfma16(af, bfr, f4{0.f, 0.f, 0.f, 0.f})[0];
        }
        LAS f4* dst = (LAS f4*)(L + M_RED + ((wave * 4 + gq) * 128 + 8 * i16) * 4);
        dst[0] = f4{pr[0], pr[1], pr[2], pr[3]};
        dst[1] = f4{pr[4], pr[5], pr[6], pr[7]};
        lds_signal(L, C_FA, lane);
        lds_wait(L, C_FA, 4);
#pragma unroll
        for (int u = 0; u < 2; ++u) {
          const int U = ks * 128 + u * 64 + lane, msd = U >> 5, b = (U >> 3) & 3, k = U & 7;
          const v2u p0 = *(const LAS v2u*)(L + M_RED + (((0 + mat) * 4 + b) * 128 + 16 * msd + 2 * k) * 4);
          const v2u p1 = *(const LAS v2u*)(L + M_RED + (((2 + mat) * 4 + b) * 128 + 16 * msd + 2 * k) * 4);
          const v4u o = {__float_as_uint(__uint_as_float(p0.x) + __uint_as_float(p1.x)), tagA, __float_as_uint(__uint_as_float(p0.y) + __uint_as_float(p1.y)), tagB};
          __builtin_amdgcn_raw_buffer_store_b128(o, rs, WS_FX + (wi * 8 + msd) * 8192 + (((xg * 2 + mat) * 4 + b) * 8 + k) * 16, 0, 16);
        }
        STAMP(12);
      }
    } else {
      const int lw = wave - 4, tl = tid - 256;
      BAR();
      {
        const u16* base = wq + (size_t)(256 * (j >> 1) + 64 * lw) * 4096 + h * 128 + 64 * (j & 1);
#pragma unroll
        for (int r = 0; r < 8; ++r) {
          const u32 row = 8 * r + (lane >> 3);
          glds16s(base, row * 8192u + (((u32)(lane & 7) ^ gsw(row)) * 16u), lds_base + L_BIG + (lw * 8 + r) * 1024);
        }
#pragma unroll
        for (int r = 0; r < 8; ++r) {
          const int row = (lw * 8 + r) * 4 + gq;
          glds16s(wuk + (size_t)(128 * j) * 4096 + h * 128, (u32)(row * 8192 + ((i16 ^ swz(row)) * 16)), lds_base + L_BIG + 32768 + (lw * 8 + r) * 1024);
        }
      }
      VMWAIT(8);
      lds_signal(L, C_T, lane);
      STAMP(1);
      __builtin_amdgcn_raw_buffer_store_b128(sv, rs, WS_INY + wid * 7168 + tl * 16, 0, 16);
      __builtin_amdgcn_raw_buffer_store_b128(sv, rs, WS_INY + wid * 7168 + (tl < 192 ? 256 + tl : tl) * 16, 0, 16);
      VMWAIT(2);
      lds_signal(L, C_T2, lane);
      VMWAIT(0);
      lds_signal(L, C_CLR, lane);
      STAMP(2);
      lds_wait(L, C_A, 4);
      STAMP(3);
#pragma unroll
      for (int r = 0; r < 8; ++r) {
        const int row = (lw * 8 + r) * 4 + gq;
        glds16s(wuv + (size_t)(128 * j) * 4096 + h * 128, (u32)(row * 8192 + ((i16 ^ swz(row)) * 16)), lds_base + L_BIG + 98304 + (lw * 8 + r) * 1024);
      }
      const u16* wop = wo + (size_t)h * 128 * 512;
#pragma unroll 4
      for (int r = 0; r < 24; ++r) {
        const int row = lw * 24 + r;
        glds16s(wop + (size_t)row * 512, (u32)((lane ^ swz(row)) * 16), lds_base + L_BIG + row * 1024);
      }
      VMWAIT(24);
      lds_signal(L, C_UV, lane);
    }
    lds_wait(L, C_UV, 4);
    v4u uvf[4];
#pragma unroll
    for (int t = 0; t < 4; ++t) {
      const int r0 = 32 * t + 8 * gq + q4, r1 = r0 + 4;
      const v2u lo = trread(L + L_BIG + 98304 + r0 * 256 + (((2 * wave + (p4 >> 1)) ^ swz(r0)) * 16) + 8 * (p4 & 1));
      const v2u hi = trread(L + L_BIG + 98304 + r1 * 256 + (((2 * wave + (p4 >> 1)) ^ swz(r1)) * 16) + 8 * (p4 & 1));
      uvf[t] = v4u{lo.x, lo.y, hi.x, hi.y};
    }
    lds_signal(L, C_UVR, lane);
    STAMP(5);
    if (wave >= 4) {
      const int lw = wave - 4;
      lds_wait(L, C_UVR, 8);
      const u16* wop = wo + (size_t)h * 128 * 512;
#pragma unroll
      for (int r = 0; r < 8; ++r) {
        const int row = 96 + lw * 8 + r;
        glds16s(wop + (size_t)row * 512, (u32)((lane ^ swz(row)) * 16), lds_base + L_BIG + row * 1024);
      }
      VMWAIT(0);
      lds_signal(L, C_WO, lane);
      STAMP(6);
    }
    v4u wf[4][4];
#define READ_WF() do { \
    _Pragma("unroll") for (int t = 0; t < 4; ++t) { \
      const int r0 = 32 * t + 8 * gq + q4, r1 = r0 + 4; \
      _Pragma("unroll") for (int tt = 0; tt < 4; ++tt) { \
        const v2u lo = trread(L + L_BIG + r0 * 1024 + (((8 * wave + 2 * tt + (p4 >> 1)) ^ swz(r0)) * 16) + 8 * (p4 & 1)); \
        const v2u hi = trread(L + L_BIG + r1 * 1024 + (((8 * wave + 2 * tt + (p4 >> 1)) ^ swz(r1)) * 16) + 8 * (p4 & 1)); \
        wf[t][tt] = v4u{lo.x, lo.y, hi.x, hi.y}; \
      } \
    } } while (0)
    bool have_wf = false;
    if (wave != 0) {
      _Pragma("unroll 1") for (u32 spins = 0; spins < (1u << 22); ++spins) {
        if (*((volatile LAS u32*)(L + M_CNT) + C_WO) >= 4u) { have_wf = true; break; }
        if (*((volatile LAS u32*)(L + M_CNT) + C_LSE) >= 1u) break;
        __builtin_amdgcn_s_sleep(0);
      }
      CFENCE();
      if (have_wf) READ_WF();
    }
    STAMP(7);
    {
      const int b = wave >> 1, chh = wave & 1, chunk = lane & 7, srow = lane >> 3;
      const u32 src = WS_IN3 + (h * 4 + j) * 32768 + (b * 32 + srow) * 256 + chh * 128 + chunk * 16;
      if (wave == 0) {
        v4u l0 = nv, l1 = nv;
        _Pragma("unroll 1") for (u32 spins = 0; spins < SPIN_CAP; ++spins) {
          CFENCE();
          if (BADTAG(l0) != 0u) l0 = __builtin_amdgcn_raw_buffer_load_b128(rs, WS_ST3 + h * 2048 + lane * 16, 0, 16);
          if (BADTAG(l1) != 0u) l1 = __builtin_amdgcn_raw_buffer_load_b128(rs, WS_ST3 + h * 2048 + 1024 + lane * 16, 0, 16);
          if (__all((BADTAG(l0) | BADTAG(l1)) == 0u)) break;
          __builtin_amdgcn_s_sleep(2);
        }
        *(LAS u32*)(L + M_LSE + lane * 4) = l0.x;
        *(LAS u32*)(L + M_LSE + 256 + lane * 4) = l1.x;
        lds_signal(L, C_LSE, lane);
      }
      lds_wait(L, C_LSE, 1);
      STAMP(10);
      v4u v[4]; float lb[4];
#pragma unroll
      for (int u = 0; u < 4; ++u) v[u] = __builtin_amdgcn_raw_buffer_load_b128(rs, src + u * 2048, 0, 16);
      __builtin_amdgcn_sched_barrier(0);
#pragma unroll
      for (int u = 0; u < 4; ++u) lb[u] = *(const LAS float*)(L + M_LSE + (b * 32 + 8 * u + srow) * 4);
      float M = fmaxf(fmaxf(lb[0], lb[1]), fmaxf(lb[2], lb[3]));
      M = fmaxf(M, dpp<0x128>(M));
      M = maxx32(maxx16(M));
      float ex[4];
#pragma unroll
      for (int u = 0; u < 4; ++u) ex[u] = __builtin_amdgcn_exp2f(lb[u] - M);
      float Ls = (ex[0] + ex[1]) + (ex[2] + ex[3]);
      Ls += dpp<0x128>(Ls);
      Ls = sumx32(sumx16(Ls));
      const float iL = __builtin_amdgcn_rcpf(Ls);
#pragma unroll
      for (int u = 0; u < 4; ++u) ex[u] *= iL;
      if (!have_wf && *((volatile LAS u32*)(L + M_CNT) + C_WO) >= 4u) { CFENCE(); READ_WF(); have_wf = true; }
      __builtin_amdgcn_sched_barrier(0);
      _Pragma("unroll 1") for (u32 spins = 0; spins < SPIN_CAP; ++spins) {
        u32 mxv = 0u;
#pragma unroll
        for (int u = 0; u < 4; ++u) { const u32 t = umax4(v[u]); mxv = mxv > t ? mxv : t; }
        if (__all(mxv != SENT)) break;
        __builtin_amdgcn_s_sleep(2);
        CFENCE();
#pragma unroll
        for (int u = 0; u < 4; ++u) if (umax4(v[u]) == SENT) v[u] = __builtin_amdgcn_raw_buffer_load_b128(rs, src + u * 2048, 0, 16);
      }
      STAMP(8);
      float oc[8];
#pragma unroll
      for (int e = 0; e < 8; ++e) oc[e] = 0.f;
#pragma unroll
      for (int u = 0; u < 4; ++u) {
        const float wv = ex[u];
        oc[0] += wv * lo_f(v[u].x); oc[1] += wv * hi_f(v[u].x); oc[2] += wv * lo_f(v[u].y); oc[3] += wv * hi_f(v[u].y);
        oc[4] += wv * lo_f(v[u].z); oc[5] += wv * hi_f(v[u].z); oc[6] += wv * lo_f(v[u].w); oc[7] += wv * hi_f(v[u].w);
      }
      float r4[4], r2[2];
#pragma unroll
      for (int e = 0; e < 4; ++e) r4[e] = tred32(oc[e], oc[4 + e]);
#pragma unroll
      for (int e = 0; e < 2; ++e) r2[e] = tred16(r4[e], r4[2 + e]);
      r2[0] += dpp<0x128>(r2[0]); r2[1] += dpp<0x128>(r2[1]);
      if ((lane & 8) == 0) {
        const u32 hp = pk_bf16(r2[0], r2[1]);
        *(LAS u32*)(L + M_OC8 + b * 272 + chh * 128 + chunk * 16 + gq * 4) = hp;
        *(LAS u32*)(L + M_OC8 + (4 + b) * 272 + chh * 128 + chunk * 16 + gq * 4) = pk_bf16(r2[0] - lo_f(hp), r2[1] - hi_f(hp));
      }
    }
    BAR();
    {
      f4 c = {0.f, 0.f, 0.f, 0.f};
#pragma unroll
      for (int t = 0; t < 4; ++t) {
        const v4u of = *(const LAS v4u*)(L + M_OC8 + (i16 & 7) * 272 + (32 * t + 8 * gq) * 2);
        c = mfma16(uvf[t], of, c);
      }
#pragma unroll
      for (int e = 0; e < 4; ++e) c[e] += dpp<0x104>(c[e]);
      if (i16 < 4) {
        const u32 h0 = pk_bf16(c[0], c[1]), h1 = pk_bf16(c[2], c[3]);
        *(LAS v2u*)(L + M_CX8 + i16 * 272 + (16 * wave + 4 * gq) * 2) = v2u{h0, h1};
        *(LAS v2u*)(L + M_CX8 + (4 + i16) * 272 + (16 * wave + 4 * gq) * 2) = v2u{pk_bf16(c[0] - lo_f(h0), c[1] - hi_f(h0)), pk_bf16(c[2] - lo_f(h1), c[3] - hi_f(h1))};
      }
    }
    if (!have_wf) READ_WF();
    BAR();
    {
      f4 sacc[4];
#pragma unroll
      for (int tt = 0; tt < 4; ++tt) sacc[tt] = f4{0.f, 0.f, 0.f, 0.f};
#pragma unroll
      for (int t = 0; t < 4; ++t) {
        const v4u cf = *(const LAS v4u*)(L + M_CX8 + (i16 & 7) * 272 + (32 * t + 8 * gq) * 2);
#pragma unroll
        for (int tt = 0; tt < 4; ++tt) sacc[tt] = mfma16(wf[t][tt], cf, sacc[tt]);
      }

      LAS unsigned char* priv = L + L_BIG + wave * 2048;
      if (i16 < 8) {
#pragma unroll
        for (int tt = 0; tt < 4; ++tt) *(LAS f4*)(priv + (i16 >> 2) * 1024 + ((tt * 4 + (i16 & 3)) * 4 + gq) * 16) = sacc[tt];
      }
      const v4u o = __builtin_bit_cast(v4u, *(const LAS f4*)(priv + lane * 16) + *(const LAS f4*)(priv + 1024 + lane * 16));
      const int f = ((lane >> 2) & 3) * 32 + 4 * wave + (lane >> 4);
      __builtin_amdgcn_raw_buffer_store_b128(nosent(o), rs, WS_IN5 + (f * 128 + wid) * 64 + (lane & 3) * 16, 0, 16);
    }
    STAMP(9);
  } else {
    const int kb = xg * 16 + idx, ab = kb >> 5, as = kb & 31;
    const bool do_ffn = idx < 14;
    const int fg = idx * 8 + xg;
    const u32 sz = swz(i16);
    v4u sf[16], vf[4][4];
    {
      const u16* kvp = ckv + ((size_t)ab * 4096 + as * 128 + (wave - 4) * 8) * 512;
      const u32 ldst = lds_base + L_BIG + (wave - 4) * 8192;
      const u32 rb = (u32)(wave & 1) * 8u;
      if (wave < 4) {
        const int ph = xg * 4 + (idx >> 2), pj = idx & 3;
        const u16* base = wq + (size_t)(256 * (pj >> 1) + 64 * wave) * 4096 + ph * 128 + 64 * (pj & 1);
#pragma unroll
        for (int r = 4; r < 8; ++r) glds16s(base, (u32)(8 * r + (lane >> 3)) * 8192u + (u32)(lane & 7) * 16u, lds_base + L_DUMMY);
#pragma unroll
        for (int r = 4; r < 8; ++r) glds16s(wuk + (size_t)(128 * pj) * 4096 + ph * 128, (u32)(((wave * 8 + r) * 4 + gq) * 8192 + i16 * 16), lds_base + L_DUMMY);
        __builtin_amdgcn_raw_buffer_store_b128(sv, rs, WS_IN5 + kb * 8192 + tid * 16, 0, 16);
        __builtin_amdgcn_raw_buffer_store_b128(sv, rs, WS_IN5 + kb * 8192 + tid * 16, 4096, 16);
      } else {
#pragma unroll
        for (int t = 0; t < KDEPTH; ++t) {
#pragma unroll
          for (int r = 0; r < 8; ++r) glds16s(kvp + (size_t)(32 * t + r) * 512, (u32)((lane ^ swz(rb + r)) * 16), ldst + (32 * t + r) * 1024);
        }
      }
      READ_EPOCH();
      if (wave == 0 && lane < 32) __builtin_amdgcn_raw_buffer_store_b128(nv, rs, WS_ST3 + ((lane * 4 + ab) * 32 + as) * 16, 0, 16);
      BAR();
#pragma unroll
      for (int t = 0; t < 4; ++t) {
        if (wave >= 4) {
          const int younger = ((t + KDEPTH < 4 ? t + KDEPTH : 4) - 1 - t) * 8;
          if (younger == 24) VMWAIT(24); else if (younger == 16) VMWAIT(16); else if (younger == 8) VMWAIT(8); else VMWAIT(0);
          lds_signal(L, C_KQ + t, lane);
          if (t + KDEPTH < 4) {
#pragma unroll
            for (int r = 0; r < 8; ++r) glds16s(kvp + (size_t)(32 * (t + KDEPTH) + r) * 512, (u32)((lane ^ swz(rb + r)) * 16), ldst + (32 * (t + KDEPTH) + r) * 1024);
          }
        }
        if (t == 3) {
#pragma unroll
          for (int e = 0; e < 4; ++e)
            __builtin_amdgcn_raw_buffer_store_b128(sv, rs, WS_IN3 + ((((8 * e + (lane >> 3)) * 4 + (wave >> 1)) * 4 + ab) * 32 + as) * 256 + (wave & 1) * 128 + (lane & 7) * 16, 0, 16);
        }
        lds_wait(L, C_KQ + t, 4);
        if (t == 3) STAMP(2);
        const int k0 = 32 * t + 8 * gq + q4, k1 = k0 + 4;
#pragma unroll
        for (int tt = 0; tt < 4; ++tt) {
          const v2u lo = trread(L + L_BIG + k0 * 1024 + (((8 * wave + 2 * tt + (p4 >> 1)) ^ swz(k0)) * 16) + 8 * (p4 & 1));
          const v2u hi = trread(L + L_BIG + k1 * 1024 + (((8 * wave + 2 * tt + (p4 >> 1)) ^ swz(k1)) * 16) + 8 * (p4 & 1));
          vf[t][tt] = v4u{lo.x, lo.y, hi.x, hi.y};
        }
        if ((wave >> 1) == t) {
          LAS unsigned char* kvrow = L + L_BIG + (16 * wave + i16) * 1024;
#pragma unroll
          for (int u = 0; u < 16; ++u) sf[u] = *(const LAS v4u*)(kvrow + 256 * (u >> 2) + 16 * ((4 * (u & 3) + gq) ^ sz));
        }
        if (t == 0) lds_signal(L, C_PR, lane);
      }
    }
    STAMP(3);
    {
      const u32 src = WS_QA + (ab * 4096 + tid) * 16;
      v4u v[8];
#pragma unroll
      for (int u = 0; u < 8; ++u) v[u] = v4u{0u, tagA ^ 1u, 0u, 0u};
      _Pragma("unroll 1") for (u32 spins = 0; spins < SPIN_CAP; ++spins) {
        CFENCE();
        v[0] = __builtin_amdgcn_raw_buffer_load_b128(rs, src, 0, 16);
        if (__all(((v[0].y ^ tagA) | (v[0].w ^ tagB)) == 0u)) break;
        __builtin_amdgcn_s_sleep(2);
      }
      _Pragma("unroll 1") for (u32 spins = 0; spins < SPIN_CAP; ++spins) {
        CFENCE();
#pragma unroll
        for (int u = 1; u < 8; ++u)
          if (((v[u].y ^ tagA) | (v[u].w ^ tagB)) != 0u) v[u] = __builtin_amdgcn_raw_buffer_load_b128(rs, src, u * 8192, 16);
        u32 bad = 0u;
#pragma unroll
        for (int u = 0; u < 8; ++u) bad |= (v[u].y ^ tagA) | (v[u].w ^ tagB);
        if (__all(bad == 0u)) break;
        __builtin_amdgcn_s_sleep(1);
      }
      const int cu = tid & 127, hb = tid >> 7;
      lds_wait(L, C_PR, 8);
#pragma unroll
      for (int u = 0; u < 8; ++u) {
        const int hh = 4 * u + hb;
        *(LAS v2u*)(L + L_BIG + hh * 1024 + (((cu >> 1) ^ swz(hh)) * 16) + (cu & 1) * 8) = v2u{v[u].x, v[u].z};
      }
    }
    STAMP(4);
    VMWAIT(0);
    BAR();
    STAMP(5);
    f4 s0 = {0.f, 0.f, 0.f, 0.f}, s1 = s0;
    {
      LAS unsigned char* qrow = L + L_BIG + i16 * 1024;
      v4u qa[4], qb[4];
#pragma unroll
      for (int t = 0; t < 4; ++t) {
        const u32 off = 16 * ((4 * t + gq) ^ sz);
        qa[t] = *(const LAS v4u*)(qrow + off); qb[t] = *(const LAS v4u*)(qrow + 16384 + off);
      }
      __builtin_amdgcn_sched_barrier(0);
#pragma unroll
      for (int t = 0; t < 16; ++t) {
        s0 = mfma16(sf[t], qa[t & 3], s0);
        s1 = mfma16(sf[t], qb[t & 3], s1);
        if (t + 4 < 16) {
          const u32 off = 256 * ((t + 4) >> 2) + 16 * ((4 * (t & 3) + gq) ^ sz);
          qa[t & 3] = *(const LAS v4u*)(qrow + off); qb[t & 3] = *(const LAS v4u*)(qrow + 16384 + off);
        }
        __builtin_amdgcn_sched_barrier(0);
      }
    }
    {
      float mx0 = maxx32(maxx16(fmaxf(fmaxf(s0[0], s0[1]), fmaxf(s0[2], s0[3]))));
      float mx1 = maxx32(maxx16(fmaxf(fmaxf(s1[0], s1[1]), fmaxf(s1[2], s1[3]))));
      if (lane < 32) *(LAS float*)(L + M_WMAX + lane * 48 + wave * 4) = lane < 16 ? mx0 : mx1;
    }
    STAMP(6);
    v4u fu = {0u, 0u, 0u, 0u}, wd0 = fu, wd1 = fu, xv = fu, wrv = fu;
    if (do_ffn) {
      fu = __builtin_amdgcn_raw_buffer_load_b128(rs, WS_FX + fg * 8192 + tid * 16, 0, 16);
      wd0 = ldnt(wd + (size_t)(16 * fg + 2 * (lane >> 3)) * 512 + 64 * wave + 8 * (lane & 7));
      wd1 = ldnt(wd + (size_t)(16 * fg + 2 * (lane >> 3) + 1) * 512 + 64 * wave + 8 * (lane & 7));
    }
    const u16 xfin = x[ab * 512 + as * 16 + (tid & 15)];
    if (wave < 4) {
      xv = *(const GAS v4u*)(x + wave * 512 + lane * 8);
      wrv = *(const GAS v4u*)(wr + lane * 8);
    }
    __builtin_amdgcn_sched_barrier(0);
    BAR();
    float m0, m1;
    {
      const f4 a0 = *(const LAS f4*)(L + M_WMAX + i16 * 48), a1 = *(const LAS f4*)(L + M_WMAX + i16 * 48 + 16);
      const f4 b0 = *(const LAS f4*)(L + M_WMAX + 768 + i16 * 48), b1 = *(const LAS f4*)(L + M_WMAX + 768 + i16 * 48 + 16);
      m0 = fmaxf(fmaxf(fmaxf(a0[0], a0[1]), fmaxf(a0[2], a0[3])), fmaxf(fmaxf(a1[0], a1[1]), fmaxf(a1[2], a1[3])));
      m1 = fmaxf(fmaxf(fmaxf(b0[0], b0[1]), fmaxf(b0[2], b0[3])), fmaxf(fmaxf(b1[0], b1[1]), fmaxf(b1[2], b1[3])));
      const u32 pa = pk_bf16(__builtin_amdgcn_exp2f(s0[0] - m0), __builtin_amdgcn_exp2f(s0[1] - m0));
      const u32 pb = pk_bf16(__builtin_amdgcn_exp2f(s0[2] - m0), __builtin_amdgcn_exp2f(s0[3] - m0));
      const u32 pc = pk_bf16(__builtin_amdgcn_exp2f(s1[0] - m1), __builtin_amdgcn_exp2f(s1[1] - m1));
      const u32 pd = pk_bf16(__builtin_amdgcn_exp2f(s1[2] - m1), __builtin_amdgcn_exp2f(s1[3] - m1));
      const u32 poff = i16 * 256 + (((2 * wave + (gq >> 1)) ^ sz) * 16) + (gq & 1) * 8;
      *(LAS v2u*)(L + L_P + poff) = v2u{pa, pb};
      *(LAS v2u*)(L + L_P + 4096 + poff) = v2u{pc, pd};
      const float l0 = sumx32(sumx16((lo_f(pa) + hi_f(pa)) + (lo_f(pb) + hi_f(pb))));
      const float l1 = sumx32(sumx16((lo_f(pc) + hi_f(pc)) + (lo_f(pd) + hi_f(pd))));
      if (lane < 32) *(LAS float*)(L + M_WSUM + lane * 48 + wave * 4) = lane < 16 ? l0 : l1;
    }
    BAR();
    STAMP(7);
    if (wave == 0 && lane < 32) {
      const f4 la = *(const LAS f4*)(L + M_WSUM + lane * 48), lb = *(const LAS f4*)(L + M_WSUM + lane * 48 + 16);
      const float l = ((la[0] + la[1]) + (la[2] + la[3])) + ((lb[0] + lb[1]) + (lb[2] + lb[3]));
      const u32 lse = __float_as_uint((lane < 16 ? m0 : m1) + __builtin_amdgcn_logf(l));
      __builtin_amdgcn_raw_buffer_store_b128(v4u{lse, tagA, lse, tagB}, rs, WS_ST3 + ((lane * 4 + ab) * 32 + as) * 16, 0, 16);
    }
#pragma unroll
    for (int ht = 0; ht < 2; ++ht) {
      f4 oacc[4];
#pragma unroll
      for (int tt = 0; tt < 4; ++tt) oacc[tt] = f4{0.f, 0.f, 0.f, 0.f};
#pragma unroll
      for (int t = 0; t < 4; ++t) {
        const v4u pf = *(const LAS v4u*)(L + L_P + ht * 4096 + i16 * 256 + (((4 * t + gq) ^ sz) * 16));
#pragma unroll
        for (int tt = 0; tt < 4; ++tt) oacc[tt] = mfma16(vf[t][tt], pf, oacc[tt]);
      }
      const f4 la = *(const LAS f4*)(L + M_WSUM + ht * 768 + i16 * 48), lb = *(const LAS f4*)(L + M_WSUM + ht * 768 + i16 * 48 + 16);
      const float l = ((la[0] + la[1]) + (la[2] + la[3])) + ((lb[0] + lb[1]) + (lb[2] + lb[3]));
      const float il = __builtin_amdgcn_rcpf(l);
      const int head = 16 * ht + i16;
      LAS unsigned char* priv = L + L_BIG + 65536 + wave * 4608;
#pragma unroll
      for (int tt = 0; tt < 4; ++tt)
        *(LAS v2u*)(priv + head * 144 + tt * 32 + gq * 8) = v2u{pk_bf16(oacc[tt][0] * il, oacc[tt][1] * il), pk_bf16(oacc[tt][2] * il, oacc[tt][3] * il)};
#pragma unroll
      for (int e = 0; e < 2; ++e) {
        const int hd = 16 * ht + 8 * e + (lane >> 3);
        const v4u o = *(const LAS v4u*)(priv + hd * 144 + (lane & 7) * 16);
        __builtin_amdgcn_raw_buffer_store_b128(nosent(o), rs,
            WS_IN3 + (((hd * 4 + (wave >> 1)) * 4 + ab) * 32 + as) * 256 + (wave & 1) * 128 + (lane & 7) * 16, 0, 16);
      }
    }
    STAMP(8);
    if (wave < 4) ln_row(L, xv, wrv, wave, lane);
    if (do_ffn) {
      const int chunk = lane & 7, rg = lane >> 3;
      _Pragma("unroll 1") for (u32 spins = 0; spins < SPIN_CAP; ++spins) {
        if (__all(((fu.y ^ tagA) | (fu.w ^ tagB)) == 0u)) break;
        CFENCE();
        fu = __builtin_amdgcn_raw_buffer_load_b128(rs, WS_FX + fg * 8192 + tid * 16, 0, 16);
      }
      *(LAS v2u*)(L + L_BIG + 32768 + tid * 8) = v2u{fu.x, fu.z};
      BAR();
      if (lane < 32) {
        float g0 = 0.f, g1 = 0.f, u0 = 0.f, u1 = 0.f;
#pragma unroll
        for (int w = 0; w < 8; ++w) {
          const v2u gv = *(const LAS v2u*)(L + L_BIG + 32768 + (w * 64 + lane) * 8), uv = *(const LAS v2u*)(L + L_BIG + 32768 + (w * 64 + 32 + lane) * 8);
          g0 += __uint_as_float(gv.x); g1 += __uint_as_float(gv.y); u0 += __uint_as_float(uv.x); u1 += __uint_as_float(uv.y);
        }
        g0 = rbf(g0); g1 = rbf(g1);
        const float h0 = rbf(rbf(g0 / (1.0f + __expf(-g0))) * rbf(u0)), h1 = rbf(rbf(g1 / (1.0f + __expf(-g1))) * rbf(u1));
        *(LAS v2u*)(L + L_BIG + 40960 + wave * 256 + lane * 8) = v2u{__float_as_uint(h0), __float_as_uint(h1)};
      }
      float y[32];
#pragma unroll
      for (int b = 0; b < 4; ++b) {
        const v2u hp = *(const LAS v2u*)(L + L_BIG + 40960 + wave * 256 + (b * 16 + 2 * rg) * 4);
        const float h0 = __uint_as_float(hp.x), h1 = __uint_as_float(hp.y);
#pragma unroll
        for (int e = 0; e < 4; ++e) {
          y[b * 8 + 2 * e] = h0 * lo_f(wd0[e]) + h1 * lo_f(wd1[e]);
          y[b * 8 + 2 * e + 1] = h0 * hi_f(wd0[e]) + h1 * hi_f(wd1[e]);
        }
      }
      float n16[16], n8[8];
#pragma unroll
      for (int k = 0; k < 16; ++k) n16[k] = tred32(y[k], y[16 + k]);
#pragma unroll
      for (int k = 0; k < 8; ++k) { n8[k] = tred16(n16[k], n16[8 + k]); n8[k] += dpp<0x128>(n8[k]); }
      LAS unsigned char* priv = L + L_BIG + 65536 + wave * 4608;
      const f4 oh = (lane & 8) ? f4{n8[4], n8[5], n8[6], n8[7]} : f4{n8[0], n8[1], n8[2], n8[3]};
      *(LAS f4*)(priv + (gq * 16 + chunk * 2 + ((lane >> 3) & 1)) * 16) = oh;
      const v4u o = *(const LAS v4u*)(priv + lane * 16);
      const int f = gq * 32 + 4 * wave + (i16 >> 2);
      __builtin_amdgcn_raw_buffer_store_b128(nosent(o), rs, WS_INY + (f * 112 + fg) * 64 + (i16 & 3) * 16, 0, 16);
    }
    STAMP(9);
    float gate = 0.f;
    {
      const u32 sy = WS_INY + kb * 7168 + (tid < 448 ? tid : 0) * 16;
      v4u y0 = sv;
      __builtin_amdgcn_s_sleep(8);
      _Pragma("unroll 1") for (u32 spins = 0; spins < SPIN_CAP; ++spins) {
        CFENCE();
        if (umax4(y0) == SENT) y0 = __builtin_amdgcn_raw_buffer_load_b128(rs, sy, 0, 16);
        if (__all(umax4(y0) != SENT)) break;
        __builtin_amdgcn_s_sleep(4);
      }
      float v[4], n2[2];
#pragma unroll
      for (int e = 0; e < 4; ++e) v[e] = tid < 448 ? __uint_as_float(y0[e]) : 0.f;
#pragma unroll
      for (int e = 0; e < 2; ++e) n2[e] = tred32(v[e], v[2 + e]);
      float n1 = tred16(n2[0], n2[1]);
      n1 += dpp<0x124>(n1); n1 += dpp<0x128>(n1);
      if (i16 < 4) *(LAS float*)(L + M_FIN + (wave * 16 + i16 * 4 + gq) * 4) = n1;
      BAR();
      if (tid < 16) {
        const int m = as * 16 + tid;
        float y = 0.f;
#pragma unroll
        for (int w = 0; w < 8; ++w) y += *(const LAS float*)(L + M_FIN + (w * 16 + tid) * 4);
        const float rsv = *(const LAS float*)(L + M_RS + ab * 4);
        const float xnv = bf2f(*(const LAS u16*)(L + L_XN + ab * XS + m * 2));
        y = rbf(rbf(y) * rsv);
        const float z = rbf(xnv + y);
        gate = rbf(z / (1.0f + __expf(-z)));
      }
    }
    STAMP(13);
    {
      const u32 s5 = WS_IN5 + kb * 8192 + tid * 16;
      v4u a0 = sv;
      _Pragma("unroll 1") for (u32 spins = 0; spins < SPIN_CAP; ++spins) {
        CFENCE();
        if (umax4(a0) == SENT) a0 = __builtin_amdgcn_raw_buffer_load_b128(rs, s5, 0, 16);
        if (__all(umax4(a0) != SENT)) break;
        __builtin_amdgcn_s_sleep(1);
      }
      STAMP(10);
      float n2[2];
#pragma unroll
      for (int e = 0; e < 2; ++e) n2[e] = tred32(__uint_as_float(a0[e]), __uint_as_float(a0[2 + e]));
      float n1 = tred16(n2[0], n2[1]);
      n1 += dpp<0x124>(n1); n1 += dpp<0x128>(n1);
      if (i16 < 4) *(LAS float*)(L + M_FIN + 512 + (wave * 16 + i16 * 4 + gq) * 4) = n1;
    }
    BAR();
    if (tid < 16) {
      float sacc = 0.f;
#pragma unroll
      for (int w = 0; w < 8; ++w) sacc += *(const LAS float*)(L + M_FIN + 512 + (w * 16 + tid) * 4);
      const float o = bf2f(xfin) + rbf(rbf(sacc) * gate);
      out[ab * 512 + as * 16 + tid] = __builtin_bit_cast(u16, (__bf16)o);
    }
    if (g == 0 && tid == 0) __builtin_amdgcn_raw_buffer_store_b64(v2u{tagA, tagB}, rs, WS_EPOCH, 0, 16);
  }
}

extern "C" void kernel_launch(void* const* d_in, const int* in_sizes, int n_in,
                              void* d_out, int out_size, void* d_ws, size_t ws_size,
                              hipStream_t stream) {
  (void)in_sizes; (void)n_in; (void)out_size;
  (void)hipFuncSetAttribute((const void*)mega, hipFuncAttributeMaxDynamicSharedMemorySize, LDS_BYTES);
  u64* st = (u64*)((char*)d_ws + ws_size - (1 << 20));
  mega<<<NWG, 512, LDS_BYTES, stream>>>((const u16*)d_in[0], (const u16*)d_in[1], (const u16*)d_in[2], (const u16*)d_in[3],
                                        (const u16*)d_in[4], (const u16*)d_in[5], (const u16*)d_in[7], (const u16*)d_in[8],
                                        (const u16*)d_in[9], (const u16*)d_in[10], (u16*)d_out, (unsigned char*)d_ws, st);
}
```

```cpp
#include <hip/hip_runtime.h>
#include <stdint.h>

typedef unsigned short u16;
typedef unsigned int u32;
typedef unsigned long long u64;
typedef u32 v4u __attribute__((ext_vector_type(4)));
typedef u32 v2u __attribute__((ext_vector_type(2)));
typedef float f4 __attribute__((ext_vector_type(4)));
typedef short bf16x8 __attribute__((ext_vector_type(8)));
typedef short s16x4 __attribute__((ext_vector_type(4)));
typedef __bf16 bf16x2 __attribute__((ext_vector_type(2)));
#define LAS __attribute__((address_space(3)))
#define GAS __attribute__((address_space(1)))

constexpr int KDEPTH = 3;
constexpr int CPF_KB = 20;
constexpr int NWG = 256;
constexpr u32 WS_EPOCH = 0;
constexpr u32 WS_QX = 4096;
constexpr u32 WS_FX = WS_QX + 32 * 8192;
constexpr u32 WS_QA = WS_FX + 112 * 8192;
constexpr u32 WS_IN3 = WS_QA + 4 * 32 * 128 * 16;
constexpr u32 WS_ST3 = WS_IN3 + 32 * 4 * 32768;
constexpr u32 WS_IN5 = WS_ST3 + 32 * 2048;
constexpr u32 WS_INY = WS_IN5 + 128 * 8192;
constexpr u32 WS_END = WS_INY + 128 * 7168;
constexpr int L_BIG = 0;
constexpr int L_XN = 131072;
constexpr int XS = 1088;
constexpr int L_P = L_XN + 4352;
constexpr int L_M = L_P + 8192;
constexpr int M_RED = L_P;
constexpr int M_QS = L_M;
constexpr int M_LSE = L_M + 1280;
constexpr int M_WMAX = L_M + 2048;
constexpr int M_WSUM = L_M + 3584;
constexpr int M_OCS = L_M + 4096;
constexpr int M_CTX = L_M + 5120;
constexpr int M_OC8 = L_M + 2048;
constexpr int M_CX8 = M_OC8 + 8 * 272;
constexpr int M_H = L_M + 6144;
constexpr int M_FIN = L_M + 6400;
constexpr int M_RS = L_M + 7424;
constexpr int M_CNT = L_M + 7488;
constexpr int L_DUMMY = L_M + 7680;
constexpr int LDS_BYTES = L_DUMMY + 1024;
enum { C_XN = 0, C_T = 1, C_RED = 2, C_CLR = 3, C_KV = 4, C_A = 5, C_UV = 6, C_UVR = 7, C_F = 8, C_FR = 9, C_H = 10, C_T2 = 11, C_QS = 12, C_LSE = 13, C_FA = 14, C_PR = 15, C_KQ = 16  , C_WO = 20 };
constexpr u32 SENT = 0xFFFFFFFFu;
constexpr u32 SPIN_CAP = 1u << 16;

__device__ __forceinline__ float bf2f(u32 v) { return __uint_as_float(v << 16); }
__device__ __forceinline__ float lo_f(u32 v) { return __uint_as_float(v << 16); }
__device__ __forceinline__ float hi_f(u32 v) { return __uint_as_float(v & 0xffff0000u); }
__device__ __forceinline__ u32 pk_bf16(float a, float b) {
  bf16x2 r; r[0] = (__bf16)a; r[1] = (__bf16)b; return __builtin_bit_cast(u32, r);
}
__device__ __forceinline__ float rbf(float a) { return (float)((__bf16)a); }
__device__ __forceinline__ f4 mfma16(v4u a, v4u b, f4 c) {
  return __builtin_amdgcn_mfma_f32_16x16x32_bf16(__builtin_bit_cast(bf16x8, a), __builtin_bit_cast(bf16x8, b), c, 0, 0, 0);
}
__device__ __forceinline__ float dot2(u32 a, u32 b, float c) {
  return __builtin_amdgcn_fdot2_f32_bf16(__builtin_bit_cast(bf16x2, a), __builtin_bit_cast(bf16x2, b), c, false);
}
__device__ __forceinline__ u32 swz(u32 row) { return ((((row >> 3) ^ (row >> 2)) & 1u) << 3) | ((row & 3u) << 1) | ((row >> 3) & 1u); }
__device__ __forceinline__ v2u trread(LAS unsigned char* p) {
  return __builtin_bit_cast(v2u, __builtin_amdgcn_ds_read_tr16_b64_v4i16((LAS s16x4*)p));
}
__device__ __forceinline__ void glds16s(const void* sbase, u32 voff, u32 lds_dst) {
  u32 keep;
  asm volatile("s_mov_b32 %0, m0\n\ts_mov_b32 m0, %3\n\ts_nop 0\n\tglobal_load_lds_dwordx4 %1, %2 nt\n\ts_mov_b32 m0, %0"
               : "=&s"(keep) : "v"(voff), "s"(sbase), "s"(lds_dst) : "memory");
}
__device__ __forceinline__ v4u ldnt(const void* p) { return __builtin_nontemporal_load((const GAS v4u*)p); }
template <int CTRL> __device__ __forceinline__ float dpp(float v) {
  return __int_as_float(__builtin_amdgcn_update_dpp(0, __float_as_int(v), CTRL, 0xf, 0xf, true));
}
__device__ __forceinline__ float sum16(float v) { v += dpp<0xB1>(v); v += dpp<0x4E>(v); v += dpp<0x141>(v); v += dpp<0x140>(v); return v; }
__device__ __forceinline__ float sumx16(float v) { auto r = __builtin_amdgcn_permlane16_swap(__float_as_uint(v), __float_as_uint(v), false, false); return __uint_as_float(r[0]) + __uint_as_float(r[1]); }
__device__ __forceinline__ float sumx32(float v) { auto r = __builtin_amdgcn_permlane32_swap(__float_as_uint(v), __float_as_uint(v), false, false); return __uint_as_float(r[0]) + __uint_as_float(r[1]); }
__device__ __forceinline__ float maxx16(float v) { auto r = __builtin_amdgcn_permlane16_swap(__float_as_uint(v), __float_as_uint(v), false, false); return fmaxf(__uint_as_float(r[0]), __uint_as_float(r[1])); }
__device__ __forceinline__ float maxx32(float v) { auto r = __builtin_amdgcn_permlane32_swap(__float_as_uint(v), __float_as_uint(v), false, false); return fmaxf(__uint_as_float(r[0]), __uint_as_float(r[1])); }
__device__ __forceinline__ float sum64(float v) { return sumx32(sumx16(sum16(v))); }
__device__ __forceinline__ float tred32(float a, float b) { auto r = __builtin_amdgcn_permlane32_swap(__float_as_uint(a), __float_as_uint(b), false, false); return __uint_as_float(r[0]) + __uint_as_float(r[1]); }
__device__ __forceinline__ float tred16(float a, float b) { auto r = __builtin_amdgcn_permlane16_swap(__float_as_uint(a), __float_as_uint(b), false, false); return __uint_as_float(r[0]) + __uint_as_float(r[1]); }

#define BAR() do { asm volatile("s_waitcnt lgkmcnt(0)" ::: "memory"); __builtin_amdgcn_s_barrier(); asm volatile("" ::: "memory"); } while (0)
#define BADTAG(v) (((v).y ^ tagA) | ((v).w ^ tagB))
#define CFENCE() asm volatile("" ::: "memory")
#define VMWAIT(n) asm volatile("s_waitcnt vmcnt(" #n ")" ::: "memory")
__device__ __forceinline__ u32 umax4(v4u v) { u32 a = v.x > v.y ? v.x : v.y, b = v.z > v.w ? v.z : v.w; return a > b ? a : b; }
__device__ __forceinline__ v4u nosent(v4u v) {
  v.x = v.x < 0xFFFFFFFEu ? v.x : 0xFFFFFFFEu; v.y = v.y < 0xFFFFFFFEu ? v.y : 0xFFFFFFFEu;
  v.z = v.z < 0xFFFFFFFEu ? v.z : 0xFFFFFFFEu; v.w = v.w < 0xFFFFFFFEu ? v.w : 0xFFFFFFFEu; return v;
}
__device__ __forceinline__ void lds_signal(LAS unsigned char* L, int c, int lane) {
  asm volatile("s_waitcnt lgkmcnt(0)" ::: "memory");
  if (lane == 0) __hip_atomic_fetch_add((LAS u32*)(L + M_CNT) + c, 1u, __ATOMIC_RELAXED, __HIP_MEMORY_SCOPE_WORKGROUP);
}
__device__ __forceinline__ void lds_wait(LAS unsigned char* L, int c, u32 target) {
  _Pragma("unroll 1") for (u32 spins = 0; spins < (1u << 22); ++spins) {
    if (*((volatile LAS u32*)(L + M_CNT) + c) >= target) break;
    __builtin_amdgcn_s_sleep(0);
  }
  CFENCE();
}

__device__ __forceinline__ void ln_row(LAS unsigned char* L, v4u xv, v4u wrv, int r, int lane) {
  float xf[8] = {lo_f(xv.x), hi_f(xv.x), lo_f(xv.y), hi_f(xv.y), lo_f(xv.z), hi_f(xv.z), lo_f(xv.w), hi_f(xv.w)};
  float s = ((xf[0] + xf[1]) + (xf[2] + xf[3])) + ((xf[4] + xf[5]) + (xf[6] + xf[7]));
  const float mean = sum64(s) * (1.0f / 512.0f);
  float q2 = 0.f;
#pragma unroll
  for (int e = 0; e < 8; ++e) { const float d = xf[e] - mean; q2 += d * d; }
  const float var = rbf(sum64(q2) * (1.0f / 512.0f));
  const float mb = rbf(mean);
  const float den = rbf(sqrtf(rbf(var + 1e-5f)));
  float xn[8];
#pragma unroll
  for (int e = 0; e < 8; ++e) xn[e] = rbf(xf[e] - mb) / den;
  v4u xnp = {pk_bf16(xn[0], xn[1]), pk_bf16(xn[2], xn[3]), pk_bf16(xn[4], xn[5]), pk_bf16(xn[6], xn[7])};
  *(LAS v4u*)(L + L_XN + r * XS + lane * 16) = xnp;
  lds_signal(L, C_XN, lane);
  float rr = dot2(xnp.x, wrv.x, 0.f); rr = dot2(xnp.y, wrv.y, rr); rr = dot2(xnp.z, wrv.z, rr); rr = dot2(xnp.w, wrv.w, rr);
  rr = rbf(sum64(rr));
  if (lane == 0) *(LAS float*)(L + M_RS + r * 4) = rbf(1.0f / (1.0f + __expf(-rr)));
}

__device__ __forceinline__ u32 gsw(u32 row) { return (((row >> 1) & 1u) | (((row >> 3) & 1u) << 1)) << 1; }
__device__ __forceinline__ void tile_gemv(LAS unsigned char* L, int tile_off, int m0, int kq, int lane) {
  const int i16 = lane & 15, gq = lane >> 4, q4 = i16 >> 2, p4 = i16 & 3;
  f4 acc[4];
#pragma unroll
  for (int dt = 0; dt < 4; ++dt) acc[dt] = f4{0.f, 0.f, 0.f, 0.f};
  const u32 fl = gsw(8 * gq + q4);
#pragma unroll
  for (int t = 0; t < 2; ++t) {
    const int r0 = 64 * kq + 32 * t + 8 * gq + q4;
    const v4u xf = *(const LAS v4u*)(L + L_XN + (i16 & 3) * XS + (m0 + 64 * kq + 32 * t + 8 * gq) * 2);
    LAS unsigned char* b0 = L + tile_off + r0 * 128 + 8 * (p4 & 1);
#pragma unroll
    for (int dt = 0; dt < 4; ++dt) {
      const v2u a0 = trread(b0 + (((2 * dt + (p4 >> 1)) ^ fl) * 16)), a1 = trread(b0 + 512 + (((2 * dt + (p4 >> 1)) ^ fl) * 16));
      acc[dt] = mfma16(v4u{a0.x, a0.y, a1.x, a1.y}, xf, acc[dt]);
    }
  }
  if (i16 < 4) {
#pragma unroll
    for (int dt = 0; dt < 4; ++dt) *(LAS f4*)(L + M_RED + (((kq * 4 + dt) * 4 + gq) * 4 + i16) * 16) = acc[dt];
  }
}

__global__ __launch_bounds__(512, 2) void mega(const u16* __restrict__ x, const u16* __restrict__ ckv, const u16* __restrict__ wq,
                                               const u16* __restrict__ wuk, const u16* __restrict__ wuv, const u16* __restrict__ wo,
                                               const u16* __restrict__ wr, const u16* __restrict__ wg, const u16* __restrict__ wu,
                                               const u16* __restrict__ wd, u16* __restrict__ out, unsigned char* ws) {
  extern __shared__ __attribute__((aligned(16))) unsigned char lds_raw[];
  LAS unsigned char* L = (LAS unsigned char*)lds_raw;
  const int tid = threadIdx.x, lane = tid & 63;
  const int wave = __builtin_amdgcn_readfirstlane(tid >> 6);
  const int g = blockIdx.x, xg = g & 7, idx = g >> 3;
  const int i16 = lane & 15, gq = lane >> 4, q4 = i16 >> 2, p4 = i16 & 3;
  const __amdgpu_buffer_rsrc_t rs = __builtin_amdgcn_make_buffer_rsrc((void*)ws, 0, (int)WS_END, 0x00020000);
  const u32 lds_base = (u32)(size_t)lds_raw;
  const v4u sv = {SENT, SENT, SENT, SENT};
  asm volatile("" :: "s"(x), "s"(ckv), "s"(wq), "s"(wuk), "s"(wuv), "s"(wo), "s"(wr), "s"(wg), "s"(wu), "s"(wd), "s"(out), "s"(ws));
  {
    u64 pc;
    asm volatile("s_getpc_b64 %0" : "=s"(pc));
    const int slice = idx;
    if (wave == 3 && slice < CPF_KB) {
      u32 keep;
      asm volatile("s_mov_b32 %0, m0\n\ts_mov_b32 m0, %3\n\ts_nop 0\n\tglobal_load_lds_dwordx4 %1, %2\n\ts_mov_b32 m0, %0"
                   : "=&s"(keep) : "v"((u32)(slice * 1024 + lane * 16)), "s"(pc & ~(u64)63), "s"(lds_base + L_DUMMY) : "memory");
    }
  }
  if (tid < 24) *((LAS u32*)(L + M_CNT) + tid) = 0u;
  u32 tagA = 0u, tagB = 0u;
#define READ_EPOCH() do { \
    v2u ep_; \
    asm volatile("s_load_dwordx2 %0, %1, 0x0\n\ts_waitcnt lgkmcnt(0)" : "=s"(ep_) : "s"(ws) : "memory"); \
    tagA = ep_.x + 1u; tagB = ep_.y + 1u; } while (0)
#define nv (v4u{0u, tagA ^ 1u, 0u, 0u})
  if (idx >= 16) {
    const int wi = idx - 16, h = xg * 4 + (wi >> 2), j = wi & 3, wid = xg * 16 + wi;
    if (wave < 4) {
      const int a = wave;
      const v4u xv = *(const GAS v4u*)(x + a * 512 + lane * 8);
      const v4u wrv = *(const GAS v4u*)(wr + lane * 8);
      BAR();
      ln_row(L, xv, wrv, a, lane);
      READ_EPOCH();
      if (lane < 32) {
        __builtin_amdgcn_raw_buffer_store_b128(nv, rs, WS_QX + (((h * 4 + a) * 2 + (j >> 1)) * 64 + 32 * (j & 1) + lane) * 16, 0, 16);
        __builtin_amdgcn_raw_buffer_store_b128(nv, rs, WS_QA + (((lane >> 3) * 32 + h) * 128 + 32 * j + 8 * a + (lane & 7)) * 16, 0, 16);
      }
      lds_wait(L, C_XN, 4);
      lds_wait(L, C_T, 4);
      tile_gemv(L, L_BIG, 256 * (j >> 1), a, lane);
      lds_signal(L, C_RED, lane);
      lds_wait(L, C_RED, 4);
      if (lane < 32) {
        float s0 = 0.f, s1 = 0.f;
#pragma unroll
        for (int kq = 0; kq < 4; ++kq) {
          const v2u pv = *(const LAS v2u*)(L + M_RED + ((kq * 16 + (lane >> 1)) * 4 + a) * 16 + (lane & 1) * 8);
          s0 += __uint_as_float(pv.x); s1 += __uint_as_float(pv.y);
        }
        __builtin_amdgcn_raw_buffer_store_b128(v4u{__float_as_uint(s0), tagA, __float_as_uint(s1), tagB}, rs,
                                               WS_QX + (((h * 4 + a) * 2 + (j >> 1)) * 64 + 32 * (j & 1) + lane) * 16, 0, 16);
      }
      lds_wait(L, C_T2, 4);
      v4u wukf[2][4];
#pragma unroll
      for (int ct = 0; ct < 2; ++ct)
#pragma unroll
        for (int t = 0; t < 4; ++t)
          wukf[ct][t] = *(const LAS v4u*)(L + L_BIG + 32768 + (32 * a + 16 * ct + i16) * 256 + (((4 * t + gq) ^ swz(i16)) * 16));
      v4u qu[2];
      qu[0] = qu[1] = v4u{0u, tagA ^ 1u, 0u, 0u};
      const u32 qsrc = WS_QX + ((h * 4 + a) * 128 + lane) * 16;
      _Pragma("unroll 1") for (u32 spins = 0; spins < SPIN_CAP; ++spins) {
        CFENCE();
#pragma unroll
        for (int u = 0; u < 2; ++u)
          if (((qu[u].y ^ tagA) | (qu[u].w ^ tagB)) != 0u) qu[u] = __builtin_amdgcn_raw_buffer_load_b128(rs, qsrc + u * 1024, 0, 16);
        const u32 bad = (qu[0].y ^ tagA) | (qu[0].w ^ tagB) | (qu[1].y ^ tagA) | (qu[1].w ^ tagB);
        if (__all(bad == 0u)) break;
      }
      *(LAS u32*)(L + M_QS + a * 320 + lane * 4) = pk_bf16(__uint_as_float(qu[0].x) + __uint_as_float(qu[1].x), __uint_as_float(qu[0].z) + __uint_as_float(qu[1].z));
      lds_signal(L, C_QS, lane);
      lds_wait(L, C_QS, 4);
      LAS unsigned char* priv = L + L_BIG + 65536 + a * 2048;
      f4 qa0 = {0.f, 0.f, 0.f, 0.f}, qa1 = qa0;
#pragma unroll
      for (int t = 0; t < 4; ++t) {
        const v4u qf = *(const LAS v4u*)(L + M_QS + (i16 & 3) * 320 + (32 * t + 8 * gq) * 2);
        qa0 = mfma16(wukf[0][t], qf, qa0);
        qa1 = mfma16(wukf[1][t], qf, qa1);
      }
      const float SC = 0.08838834764831845f * 1.4426950408889634f;
      if (i16 < 4) {
        *(LAS v2u*)(priv + 1024 + (i16 * 8 + gq) * 8) = v2u{pk_bf16(qa0[0] * SC, qa0[1] * SC), pk_bf16(qa0[2] * SC, qa0[3] * SC)};
        *(LAS v2u*)(priv + 1024 + (i16 * 8 + 4 + gq) * 8) = v2u{pk_bf16(qa1[0] * SC, qa1[1] * SC), pk_bf16(qa1[2] * SC, qa1[3] * SC)};
      }
      lds_wait(L, C_CLR, 4);
      if (lane < 32) {
        const v2u pv = *(const LAS v2u*)(priv + 1024 + lane * 8);
        __builtin_amdgcn_raw_buffer_store_b128(v4u{pv.x, tagA, pv.y, tagB}, rs, WS_QA + (((lane >> 3) * 32 + h) * 128 + 32 * j + 8 * a + (lane & 7)) * 16, 0, 16);
      }
      lds_signal(L, C_A, lane);
      if (wi < 14) {
        v4u fw[8];
        const int mat = wave & 1, ks = wave >> 1;
        const u16* src = (mat ? wu : wg) + (size_t)(64 * xg + 32 * ks + 8 * gq) * 1792 + 128 * wi + 8 * i16;
#pragma unroll
        for (int jj = 0; jj < 8; ++jj) fw[jj] = ldnt(src + (size_t)jj * 1792);
        const v4u af = *(const LAS v4u*)(L + L_XN + (i16 >> 2) * XS + (64 * xg + 32 * ks + 8 * gq) * 2);
        float pr[8];
#pragma unroll
        for (int cc = 0; cc < 8; ++cc) {
          const u32 sel = (cc & 1) ? 0x07060302u : 0x05040100u;
          v4u bfr;
          bfr.x = __builtin_amdgcn_perm(fw[1][cc >> 1], fw[0][cc >> 1], sel);
          bfr.y = __builtin_amdgcn_perm(fw[3][cc >> 1], fw[2][cc >> 1], sel);
          bfr.z = __builtin_amdgcn_perm(fw[5][cc >> 1], fw[4][cc >> 1], sel);
          bfr.w = __builtin_amdgcn_perm(fw[7][cc >> 1], fw[6][cc >> 1], sel);
          pr[cc] = mfma16(af, bfr, f4{0.f, 0.f, 0.f, 0.f})[0];
        }
        LAS f4* dst = (LAS f4*)(L + M_RED + ((wave * 4 + gq) * 128 + 8 * i16) * 4);
        dst[0] = f4{pr[0], pr[1], pr[2], pr[3]};
        dst[1] = f4{pr[4], pr[5], pr[6], pr[7]};
        lds_signal(L, C_FA, lane);
        lds_wait(L, C_FA, 4);
#pragma unroll
        for (int u = 0; u < 2; ++u) {
          const int U = ks * 128 + u * 64 + lane, msd = U >> 5, b = (U >> 3) & 3, k = U & 7;
          const v2u p0 = *(const LAS v2u*)(L + M_RED + (((0 + mat) * 4 + b) * 128 + 16 * msd + 2 * k) * 4);
          const v2u p1 = *(const LAS v2u*)(L + M_RED + (((2 + mat) * 4 + b) * 128 + 16 * msd + 2 * k) * 4);
          const v4u o = {__float_as_uint(__uint_as_float(p0.x) + __uint_as_float(p1.x)), tagA, __float_as_uint(__uint_as_float(p0.y) + __uint_as_float(p1.y)), tagB};
          __builtin_amdgcn_raw_buffer_store_b128(o, rs, WS_FX + (wi * 8 + msd) * 8192 + (((xg * 2 + mat) * 4 + b) * 8 + k) * 16, 0, 16);
        }
      }
    } else {
      const int lw = wave - 4, tl = tid - 256;
      BAR();
      {
        const u16* base = wq + (size_t)(256 * (j >> 1) + 64 * lw) * 4096 + h * 128 + 64 * (j & 1);
#pragma unroll
        for (int r = 0; r < 8; ++r) {
          const u32 row = 8 * r + (lane >> 3);
          glds16s(base, row * 8192u + (((u32)(lane & 7) ^ gsw(row)) * 16u), lds_base + L_BIG + (lw * 8 + r) * 1024);
        }
#pragma unroll
        for (int r = 0; r < 8; ++r) {
          const int row = (lw * 8 + r) * 4 + gq;
          glds16s(wuk + (size_t)(128 * j) * 4096 + h * 128, (u32)(row * 8192 + ((i16 ^ swz(row)) * 16)), lds_base + L_BIG + 32768 + (lw * 8 + r) * 1024);
        }
      }
      VMWAIT(8);
      lds_signal(L, C_T, lane);
      __builtin_amdgcn_raw_buffer_store_b128(sv, rs, WS_INY + wid * 7168 + tl * 16, 0, 16);
      __builtin_amdgcn_raw_buffer_store_b128(sv, rs, WS_INY + wid * 7168 + (tl < 192 ? 256 + tl : tl) * 16, 0, 16);
      VMWAIT(2);
      lds_signal(L, C_T2, lane);
      VMWAIT(0);
      lds_signal(L, C_CLR, lane);
      lds_wait(L, C_A, 4);
#pragma unroll
      for (int r = 0; r < 8; ++r) {
        const int row = (lw * 8 + r) * 4 + gq;
        glds16s(wuv + (size_t)(128 * j) * 4096 + h * 128, (u32)(row * 8192 + ((i16 ^ swz(row)) * 16)), lds_base + L_BIG + 98304 + (lw * 8 + r) * 1024);
      }
      const u16* wop = wo + (size_t)h * 128 * 512;
#pragma unroll 4
      for (int r = 0; r < 24; ++r) {
        const int row = lw * 24 + r;
        glds16s(wop + (size_t)row * 512, (u32)((lane ^ swz(row)) * 16), lds_base + L_BIG + row * 1024);
      }
      VMWAIT(24);
      lds_signal(L, C_UV, lane);
    }
    lds_wait(L, C_UV, 4);
    v4u uvf[4];
#pragma unroll
    for (int t = 0; t < 4; ++t) {
      const int r0 = 32 * t + 8 * gq + q4, r1 = r0 + 4;
      const v2u lo = trread(L + L_BIG + 98304 + r0 * 256 + (((2 * wave + (p4 >> 1)) ^ swz(r0)) * 16) + 8 * (p4 & 1));
      const v2u hi = trread(L + L_BIG + 98304 + r1 * 256 + (((2 * wave + (p4 >> 1)) ^ swz(r1)) * 16) + 8 * (p4 & 1));
      uvf[t] = v4u{lo.x, lo.y, hi.x, hi.y};
    }
    lds_signal(L, C_UVR, lane);
    if (wave >= 4) {
      const int lw = wave - 4;
      lds_wait(L, C_UVR, 8);
      const u16* wop = wo + (size_t)h * 128 * 512;
#pragma unroll
      for (int r = 0; r < 8; ++r) {
        const int row = 96 + lw * 8 + r;
        glds16s(wop + (size_t)row * 512, (u32)((lane ^ swz(row)) * 16), lds_base + L_BIG + row * 1024);
      }
      VMWAIT(0);
      lds_signal(L, C_WO, lane);
    }
    v4u wf[4][4];
#define READ_WF() do { \
    _Pragma("unroll") for (int t = 0; t < 4; ++t) { \
      const int r0 = 32 * t + 8 * gq + q4, r1 = r0 + 4; \
      _Pragma("unroll") for (int tt = 0; tt < 4; ++tt) { \
        const v2u lo = trread(L + L_BIG + r0 * 1024 + (((8 * wave + 2 * tt + (p4 >> 1)) ^ swz(r0)) * 16) + 8 * (p4 & 1)); \
        const v2u hi = trread(L + L_BIG + r1 * 1024 + (((8 * wave + 2 * tt + (p4 >> 1)) ^ swz(r1)) * 16) + 8 * (p4 & 1)); \
        wf[t][tt] = v4u{lo.x, lo.y, hi.x, hi.y}; \
      } \
    } } while (0)
    bool have_wf = false;
    if (wave != 0) {
      _Pragma("unroll 1") for (u32 spins = 0; spins < (1u << 22); ++spins) {
        if (*((volatile LAS u32*)(L + M_CNT) + C_WO) >= 4u) { have_wf = true; break; }
        if (*((volatile LAS u32*)(L + M_CNT) + C_LSE) >= 1u) break;
        __builtin_amdgcn_s_sleep(0);
      }
      CFENCE();
      if (have_wf) READ_WF();
    }
    {
      const int b = wave >> 1, chh = wave & 1, chunk = lane & 7, srow = lane >> 3;
      const u32 src = WS_IN3 + (h * 4 + j) * 32768 + (b * 32 + srow) * 256 + chh * 128 + chunk * 16;
      if (wave == 0) {
        v4u l0 = nv, l1 = nv;
        _Pragma("unroll 1") for (u32 spins = 0; spins < SPIN_CAP; ++spins) {
          CFENCE();
          if (BADTAG(l0) != 0u) l0 = __builtin_amdgcn_raw_buffer_load_b128(rs, WS_ST3 + h * 2048 + lane * 16, 0, 16);
          if (BADTAG(l1) != 0u) l1 = __builtin_amdgcn_raw_buffer_load_b128(rs, WS_ST3 + h * 2048 + 1024 + lane * 16, 0, 16);
          if (__all((BADTAG(l0) | BADTAG(l1)) == 0u)) break;
          __builtin_amdgcn_s_sleep(2);
        }
        *(LAS u32*)(L + M_LSE + lane * 4) = l0.x;
        *(LAS u32*)(L + M_LSE + 256 + lane * 4) = l1.x;
        lds_signal(L, C_LSE, lane);
      }
      lds_wait(L, C_LSE, 1);
      v4u v[4]; float lb[4];
#pragma unroll
      for (int u = 0; u < 4; ++u) v[u] = __builtin_amdgcn_raw_buffer_load_b128(rs, src + u * 2048, 0, 16);
      __builtin_amdgcn_sched_barrier(0);
#pragma unroll
      for (int u = 0; u < 4; ++u) lb[u] = *(const LAS float*)(L + M_LSE + (b * 32 + 8 * u + srow) * 4);
      float M = fmaxf(fmaxf(lb[0], lb[1]), fmaxf(lb[2], lb[3]));
      M = fmaxf(M, dpp<0x128>(M));
      M = maxx32(maxx16(M));
      float ex[4];
#pragma unroll
      for (int u = 0; u < 4; ++u) ex[u] = __builtin_amdgcn_exp2f(lb[u] - M);
      float Ls = (ex[0] + ex[1]) + (ex[2] + ex[3]);
      Ls += dpp<0x128>(Ls);
      Ls = sumx32(sumx16(Ls));
      const float iL = __builtin_amdgcn_rcpf(Ls);
#pragma unroll
      for (int u = 0; u < 4; ++u) ex[u] *= iL;
      if (!have_wf && *((volatile LAS u32*)(L + M_CNT) + C_WO) >= 4u) { CFENCE(); READ_WF(); have_wf = true; }
      __builtin_amdgcn_sched_barrier(0);
      _Pragma("unroll 1") for (u32 spins = 0; spins < SPIN_CAP; ++spins) {
        u32 mxv = 0u;
#pragma unroll
        for (int u = 0; u < 4; ++u) { const u32 t = umax4(v[u]); mxv = mxv > t ? mxv : t; }
        if (__all(mxv != SENT)) break;
        __builtin_amdgcn_s_sleep(2);
        CFENCE();
#pragma unroll
        for (int u = 0; u < 4; ++u) if (umax4(v[u]) == SENT) v[u] = __builtin_amdgcn_raw_buffer_load_b128(rs, src + u * 2048, 0, 16);
      }
      float oc[8];
#pragma unroll
      for (int e = 0; e < 8; ++e) oc[e] = 0.f;
#pragma unroll
      for (int u = 0; u < 4; ++u) {
        const float wv = ex[u];
        oc[0] += wv * lo_f(v[u].x); oc[1] += wv * hi_f(v[u].x); oc[2] += wv * lo_f(v[u].y); oc[3] += wv * hi_f(v[u].y);
        oc[4] += wv * lo_f(v[u].z); oc[5] += wv * hi_f(v[u].z); oc[6] += wv * lo_f(v[u].w); oc[7] += wv * hi_f(v[u].w);
      }
      float r4[4], r2[2];
#pragma unroll
      for (int e = 0; e < 4; ++e) r4[e] = tred32(oc[e], oc[4 + e]);
#pragma unroll
      for (int e = 0; e < 2; ++e) r2[e] = tred16(r4[e], r4[2 + e]);
      r2[0] += dpp<0x128>(r2[0]); r2[1] += dpp<0x128>(r2[1]);
      if ((lane & 8) == 0) {
        const u32 hp = pk_bf16(r2[0], r2[1]);
        *(LAS u32*)(L + M_OC8 + b * 272 + chh * 128 + chunk * 16 + gq * 4) = hp;
        *(LAS u32*)(L + M_OC8 + (4 + b) * 272 + chh * 128 + chunk * 16 + gq * 4) = pk_bf16(r2[0] - lo_f(hp), r2[1] - hi_f(hp));
      }
    }
    BAR();
    {
      f4 c = {0.f, 0.f, 0.f, 0.f};
#pragma unroll
      for (int t = 0; t < 4; ++t) {
        const v4u of = *(const LAS v4u*)(L + M_OC8 + (i16 & 7) * 272 + (32 * t + 8 * gq) * 2);
        c = mfma16(uvf[t], of, c);
      }
#pragma unroll
      for (int e = 0; e < 4; ++e) c[e] += dpp<0x104>(c[e]);
      if (i16 < 4) {
        const u32 h0 = pk_bf16(c[0], c[1]), h1 = pk_bf16(c[2], c[3]);
        *(LAS v2u*)(L + M_CX8 + i16 * 272 + (16 * wave + 4 * gq) * 2) = v2u{h0, h1};
        *(LAS v2u*)(L + M_CX8 + (4 + i16) * 272 + (16 * wave + 4 * gq) * 2) = v2u{pk_bf16(c[0] - lo_f(h0), c[1] - hi_f(h0)), pk_bf16(c[2] - lo_f(h1), c[3] - hi_f(h1))};
      }
    }
    if (!have_wf) READ_WF();
    BAR();
    {
      f4 sacc[4];
#pragma unroll
      for (int tt = 0; tt < 4; ++tt) sacc[tt] = f4{0.f, 0.f, 0.f, 0.f};
#pragma unroll
      for (int t = 0; t < 4; ++t) {
        const v4u cf = *(const LAS v4u*)(L + M_CX8 + (i16 & 7) * 272 + (32 * t + 8 * gq) * 2);
#pragma unroll
        for (int tt = 0; tt < 4; ++tt) sacc[tt] = mfma16(wf[t][tt], cf, sacc[tt]);
      }

      LAS unsigned char* priv = L + L_BIG + wave * 2048;
      if (i16 < 8) {
#pragma unroll
        for (int tt = 0; tt < 4; ++tt) *(LAS f4*)(priv + (i16 >> 2) * 1024 + ((tt * 4 + (i16 & 3)) * 4 + gq) * 16) = sacc[tt];
      }
      const v4u o = __builtin_bit_cast(v4u, *(const LAS f4*)(priv + lane * 16) + *(const LAS f4*)(priv + 1024 + lane * 16));
      const int f = ((lane >> 2) & 3) * 32 + 4 * wave + (lane >> 4);
      __builtin_amdgcn_raw_buffer_store_b128(nosent(o), rs, WS_IN5 + (f * 128 + wid) * 64 + (lane & 3) * 16, 0, 16);
    }
  } else {
    const int kb = xg * 16 + idx, ab = kb >> 5, as = kb & 31;
    const bool do_ffn = idx < 14;
    const int fg = idx * 8 + xg;
    const u32 sz = swz(i16);
    v4u sf[16], vf[4][4];
    {
      const u16* kvp = ckv + ((size_t)ab * 4096 + as * 128 + (wave - 4) * 8) * 512;
      const u32 ldst = lds_base + L_BIG + (wave - 4) * 8192;
      const u32 rb = (u32)(wave & 1) * 8u;
      if (wave < 4) {
        const int ph = xg * 4 + (idx >> 2), pj = idx & 3;
        const u16* base = wq + (size_t)(256 * (pj >> 1) + 64 * wave) * 4096 + ph * 128 + 64 * (pj & 1);
#pragma unroll
        for (int r = 4; r < 8; ++r) glds16s(base, (u32)(8 * r + (lane >> 3)) * 8192u + (u32)(lane & 7) * 16u, lds_base + L_DUMMY);
#pragma unroll
        for (int r = 4; r < 8; ++r) glds16s(wuk + (size_t)(128 * pj) * 4096 + ph * 128, (u32)(((wave * 8 + r) * 4 + gq) * 8192 + i16 * 16), lds_base + L_DUMMY);
        __builtin_amdgcn_raw_buffer_store_b128(sv, rs, WS_IN5 + kb * 8192 + tid * 16, 0, 16);
        __builtin_amdgcn_raw_buffer_store_b128(sv, rs, WS_IN5 + kb * 8192 + tid * 16, 4096, 16);
      } else {
#pragma unroll
        for (int t = 0; t < KDEPTH; ++t) {
#pragma unroll
          for (int r = 0; r < 8; ++r) glds16s(kvp + (size_t)(32 * t + r) * 512, (u32)((lane ^ swz(rb + r)) * 16), ldst + (32 * t + r) * 1024);
        }
      }
      READ_EPOCH();
      if (wave == 0 && lane < 32) __builtin_amdgcn_raw_buffer_store_b128(nv, rs, WS_ST3 + ((lane * 4 + ab) * 32 + as) * 16, 0, 16);
      BAR();
#pragma unroll
      for (int t = 0; t < 4; ++t) {
        if (wave >= 4) {
          const int younger = ((t + KDEPTH < 4 ? t + KDEPTH : 4) - 1 - t) * 8;
          if (younger == 24) VMWAIT(24); else if (younger == 16) VMWAIT(16); else if (younger == 8) VMWAIT(8); else VMWAIT(0);
          lds_signal(L, C_KQ + t, lane);
          if (t + KDEPTH < 4) {
#pragma unroll
            for (int r = 0; r < 8; ++r) glds16s(kvp + (size_t)(32 * (t + KDEPTH) + r) * 512, (u32)((lane ^ swz(rb + r)) * 16), ldst + (32 * (t + KDEPTH) + r) * 1024);
          }
        }
        if (t == 3) {
#pragma unroll
          for (int e = 0; e < 4; ++e)
            __builtin_amdgcn_raw_buffer_store_b128(sv, rs, WS_IN3 + ((((8 * e + (lane >> 3)) * 4 + (wave >> 1)) * 4 + ab) * 32 + as) * 256 + (wave & 1) * 128 + (lane & 7) * 16, 0, 16);
        }
        lds_wait(L, C_KQ + t, 4);
        const int k0 = 32 * t + 8 * gq + q4, k1 = k0 + 4;
#pragma unroll
        for (int tt = 0; tt < 4; ++tt) {
          const v2u lo = trread(L + L_BIG + k0 * 1024 + (((8 * wave + 2 * tt + (p4 >> 1)) ^ swz(k0)) * 16) + 8 * (p4 & 1));
          const v2u hi = trread(L + L_BIG + k1 * 1024 + (((8 * wave + 2 * tt + (p4 >> 1)) ^ swz(k1)) * 16) + 8 * (p4 & 1));
          vf[t][tt] = v4u{lo.x, lo.y, hi.x, hi.y};
        }
        if ((wave >> 1) == t) {
          LAS unsigned char* kvrow = L + L_BIG + (16 * wave + i16) * 1024;
#pragma unroll
          for (int u = 0; u < 16; ++u) sf[u] = *(const LAS v4u*)(kvrow + 256 * (u >> 2) + 16 * ((4 * (u & 3) + gq) ^ sz));
        }
        if (t == 0) lds_signal(L, C_PR, lane);
      }
    }
    {
      const u32 src = WS_QA + (ab * 4096 + tid) * 16;
      v4u v[8];
#pragma unroll
      for (int u = 0; u < 8; ++u) v[u] = v4u{0u, tagA ^ 1u, 0u, 0u};
      _Pragma("unroll 1") for (u32 spins = 0; spins < SPIN_CAP; ++spins) {
        CFENCE();
        v[0] = __builtin_amdgcn_raw_buffer_load_b128(rs, src, 0, 16);
        if (__all(((v[0].y ^ tagA) | (v[0].w ^ tagB)) == 0u)) break;
        __builtin_amdgcn_s_sleep(2);
      }
      _Pragma("unroll 1") for (u32 spins = 0; spins < SPIN_CAP; ++spins) {
        CFENCE();
#pragma unroll
        for (int u = 1; u < 8; ++u)
          if (((v[u].y ^ tagA) | (v[u].w ^ tagB)) != 0u) v[u] = __builtin_amdgcn_raw_buffer_load_b128(rs, src, u * 8192, 16);
        u32 bad = 0u;
#pragma unroll
        for (int u = 0; u < 8; ++u) bad |= (v[u].y ^ tagA) | (v[u].w ^ tagB);
        if (__all(bad == 0u)) break;
        __builtin_amdgcn_s_sleep(1);
      }
      const int cu = tid & 127, hb = tid >> 7;
      lds_wait(L, C_PR, 8);
#pragma unroll
      for (int u = 0; u < 8; ++u) {
        const int hh = 4 * u + hb;
        *(LAS v2u*)(L + L_BIG + hh * 1024 + (((cu >> 1) ^ swz(hh)) * 16) + (cu & 1) * 8) = v2u{v[u].x, v[u].z};
      }
    }
    VMWAIT(0);
    BAR();
    f4 s0 = {0.f, 0.f, 0.f, 0.f}, s1 = s0;
    {
      LAS unsigned char* qrow = L + L_BIG + i16 * 1024;
      v4u qa[4], qb[4];
#pragma unroll
      for (int t = 0; t < 4; ++t) {
        const u32 off = 16 * ((4 * t + gq) ^ sz);
        qa[t] = *(const LAS v4u*)(qrow + off); qb[t] = *(const LAS v4u*)(qrow + 16384 + off);
      }
      __builtin_amdgcn_sched_barrier(0);
#pragma unroll
      for (int t = 0; t < 16; ++t) {
        s0 = mfma16(sf[t], qa[t & 3], s0);
        s1 = mfma16(sf[t], qb[t & 3], s1);
        if (t + 4 < 16) {
          const u32 off = 256 * ((t + 4) >> 2) + 16 * ((4 * (t & 3) + gq) ^ sz);
          qa[t & 3] = *(const LAS v4u*)(qrow + off); qb[t & 3] = *(const LAS v4u*)(qrow + 16384 + off);
        }
        __builtin_amdgcn_sched_barrier(0);
      }
    }
    {
      float mx0 = maxx32(maxx16(fmaxf(fmaxf(s0[0], s0[1]), fmaxf(s0[2], s0[3]))));
      float mx1 = maxx32(maxx16(fmaxf(fmaxf(s1[0], s1[1]), fmaxf(s1[2], s1[3]))));
      if (lane < 32) *(LAS float*)(L + M_WMAX + lane * 48 + wave * 4) = lane < 16 ? mx0 : mx1;
    }
    v4u fu = {0u, 0u, 0u, 0u}, wd0 = fu, wd1 = fu, xv = fu, wrv = fu;
    if (do_ffn) {
      fu = __builtin_amdgcn_raw_buffer_load_b128(rs, WS_FX + fg * 8192 + tid * 16, 0, 16);
      wd0 = ldnt(wd + (size_t)(16 * fg + 2 * (lane >> 3)) * 512 + 64 * wave + 8 * (lane & 7));
      wd1 = ldnt(wd + (size_t)(16 * fg + 2 * (lane >> 3) + 1) * 512 + 64 * wave + 8 * (lane & 7));
    }
    const u16 xfin = x[ab * 512 + as * 16 + (tid & 15)];
    if (wave < 4) {
      xv = *(const GAS v4u*)(x + wave * 512 + lane * 8);
      wrv = *(const GAS v4u*)(wr + lane * 8);
    }
    __builtin_amdgcn_sched_barrier(0);
    BAR();
    float m0, m1;
    {
      const f4 a0 = *(const LAS f4*)(L + M_WMAX + i16 * 48), a1 = *(const LAS f4*)(L + M_WMAX + i16 * 48 + 16);
      const f4 b0 = *(const LAS f4*)(L + M_WMAX + 768 + i16 * 48), b1 = *(const LAS f4*)(L + M_WMAX + 768 + i16 * 48 + 16);
      m0 = fmaxf(fmaxf(fmaxf(a0[0], a0[1]), fmaxf(a0[2], a0[3])), fmaxf(fmaxf(a1[0], a1[1]), fmaxf(a1[2], a1[3])));
      m1 = fmaxf(fmaxf(fmaxf(b0[0], b0[1]), fmaxf(b0[2], b0[3])), fmaxf(fmaxf(b1[0], b1[1]), fmaxf(b1[2], b1[3])));
      const u32 pa = pk_bf16(__builtin_amdgcn_exp2f(s0[0] - m0), __builtin_amdgcn_exp2f(s0[1] - m0));
      const u32 pb = pk_bf16(__builtin_amdgcn_exp2f(s0[2] - m0), __builtin_amdgcn_exp2f(s0[3] - m0));
      const u32 pc = pk_bf16(__builtin_amdgcn_exp2f(s1[0] - m1), __builtin_amdgcn_exp2f(s1[1] - m1));
      const u32 pd = pk_bf16(__builtin_amdgcn_exp2f(s1[2] - m1), __builtin_amdgcn_exp2f(s1[3] - m1));
      const u32 poff = i16 * 256 + (((2 * wave + (gq >> 1)) ^ sz) * 16) + (gq & 1) * 8;
      *(LAS v2u*)(L + L_P + poff) = v2u{pa, pb};
      *(LAS v2u*)(L + L_P + 4096 + poff) = v2u{pc, pd};
      const float l0 = sumx32(sumx16((lo_f(pa) + hi_f(pa)) + (lo_f(pb) + hi_f(pb))));
      const float l1 = sumx32(sumx16((lo_f(pc) + hi_f(pc)) + (lo_f(pd) + hi_f(pd))));
      if (lane < 32) *(LAS float*)(L + M_WSUM + lane * 48 + wave * 4) = lane < 16 ? l0 : l1;
    }
    BAR();
    if (wave == 0 && lane < 32) {
      const f4 la = *(const LAS f4*)(L + M_WSUM + lane * 48), lb = *(const LAS f4*)(L + M_WSUM + lane * 48 + 16);
      const float l = ((la[0] + la[1]) + (la[2] + la[3])) + ((lb[0] + lb[1]) + (lb[2] + lb[3]));
      const u32 lse = __float_as_uint((lane < 16 ? m0 : m1) + __builtin_amdgcn_logf(l));
      __builtin_amdgcn_raw_buffer_store_b128(v4u{lse, tagA, lse, tagB}, rs, WS_ST3 + ((lane * 4 + ab) * 32 + as) * 16, 0, 16);
    }
#pragma unroll
    for (int ht = 0; ht < 2; ++ht) {
      f4 oacc[4];
#pragma unroll
      for (int tt = 0; tt < 4; ++tt) oacc[tt] = f4{0.f, 0.f, 0.f, 0.f};
#pragma unroll
      for (int t = 0; t < 4; ++t) {
        const v4u pf = *(const LAS v4u*)(L + L_P + ht * 4096 + i16 * 256 + (((4 * t + gq) ^ sz) * 16));
#pragma unroll
        for (int tt = 0; tt < 4; ++tt) oacc[tt] = mfma16(vf[t][tt], pf, oacc[tt]);
      }
      const f4 la = *(const LAS f4*)(L + M_WSUM + ht * 768 + i16 * 48), lb = *(const LAS f4*)(L + M_WSUM + ht * 768 + i16 * 48 + 16);
      const float l = ((la[0] + la[1]) + (la[2] + la[3])) + ((lb[0] + lb[1]) + (lb[2] + lb[3]));
      const float il = __builtin_amdgcn_rcpf(l);
      const int head = 16 * ht + i16;
      LAS unsigned char* priv = L + L_BIG + 65536 + wave * 4608;
#pragma unroll
      for (int tt = 0; tt < 4; ++tt)
        *(LAS v2u*)(priv + head * 144 + tt * 32 + gq * 8) = v2u{pk_bf16(oacc[tt][0] * il, oacc[tt][1] * il), pk_bf16(oacc[tt][2] * il, oacc[tt][3] * il)};
#pragma unroll
      for (int e = 0; e < 2; ++e) {
        const int hd = 16 * ht + 8 * e + (lane >> 3);
        const v4u o = *(const LAS v4u*)(priv + hd * 144 + (lane & 7) * 16);
        __builtin_amdgcn_raw_buffer_store_b128(nosent(o), rs,
            WS_IN3 + (((hd * 4 + (wave >> 1)) * 4 + ab) * 32 + as) * 256 + (wave & 1) * 128 + (lane & 7) * 16, 0, 16);
      }
    }
    if (wave < 4) ln_row(L, xv, wrv, wave, lane);
    if (do_ffn) {
      const int chunk = lane & 7, rg = lane >> 3;
      _Pragma("unroll 1") for (u32 spins = 0; spins < SPIN_CAP; ++spins) {
        if (__all(((fu.y ^ tagA) | (fu.w ^ tagB)) == 0u)) break;
        CFENCE();
        fu = __builtin_amdgcn_raw_buffer_load_b128(rs, WS_FX + fg * 8192 + tid * 16, 0, 16);
      }
      *(LAS v2u*)(L + L_BIG + 32768 + tid * 8) = v2u{fu.x, fu.z};
      BAR();
      if (lane < 32) {
        float g0 = 0.f, g1 = 0.f, u0 = 0.f, u1 = 0.f;
#pragma unroll
        for (int w = 0; w < 8; ++w) {
          const v2u gv = *(const LAS v2u*)(L + L_BIG + 32768 + (w * 64 + lane) * 8), uv = *(const LAS v2u*)(L + L_BIG + 32768 + (w * 64 + 32 + lane) * 8);
          g0 += __uint_as_float(gv.x); g1 += __uint_as_float(gv.y); u0 += __uint_as_float(uv.x); u1 += __uint_as_float(uv.y);
        }
        g0 = rbf(g0); g1 = rbf(g1);
        const float h0 = rbf(rbf(g0 / (1.0f + __expf(-g0))) * rbf(u0)), h1 = rbf(rbf(g1 / (1.0f + __expf(-g1))) * rbf(u1));
        *(LAS v2u*)(L + L_BIG + 40960 + wave * 256 + lane * 8) = v2u{__float_as_uint(h0), __float_as_uint(h1)};
      }
      float y[32];
#pragma unroll
      for (int b = 0; b < 4; ++b) {
        const v2u hp = *(const LAS v2u*)(L + L_BIG + 40960 + wave * 256 + (b * 16 + 2 * rg) * 4);
        const float h0 = __uint_as_float(hp.x), h1 = __uint_as_float(hp.y);
#pragma unroll
        for (int e = 0; e < 4; ++e) {
          y[b * 8 + 2 * e] = h0 * lo_f(wd0[e]) + h1 * lo_f(wd1[e]);
          y[b * 8 + 2 * e + 1] = h0 * hi_f(wd0[e]) + h1 * hi_f(wd1[e]);
        }
      }
      float n16[16], n8[8];
#pragma unroll
      for (int k = 0; k < 16; ++k) n16[k] = tred32(y[k], y[16 + k]);
#pragma unroll
      for (int k = 0; k < 8; ++k) { n8[k] = tred16(n16[k], n16[8 + k]); n8[k] += dpp<0x128>(n8[k]); }
      LAS unsigned char* priv = L + L_BIG + 65536 + wave * 4608;
      const f4 oh = (lane & 8) ? f4{n8[4], n8[5], n8[6], n8[7]} : f4{n8[0], n8[1], n8[2], n8[3]};
      *(LAS f4*)(priv + (gq * 16 + chunk * 2 + ((lane >> 3) & 1)) * 16) = oh;
      const v4u o = *(const LAS v4u*)(priv + lane * 16);
      const int f = gq * 32 + 4 * wave + (i16 >> 2);
      __builtin_amdgcn_raw_buffer_store_b128(nosent(o), rs, WS_INY + (f * 112 + fg) * 64 + (i16 & 3) * 16, 0, 16);
    }
    float gate = 0.f;
    {
      const u32 sy = WS_INY + kb * 7168 + (tid < 448 ? tid : 0) * 16;
      v4u y0 = sv;
      __builtin_amdgcn_s_sleep(8);
      _Pragma("unroll 1") for (u32 spins = 0; spins < SPIN_CAP; ++spins) {
        CFENCE();
        if (umax4(y0) == SENT) y0 = __builtin_amdgcn_raw_buffer_load_b128(rs, sy, 0, 16);
        if (__all(umax4(y0) != SENT)) break;
        __builtin_amdgcn_s_sleep(4);
      }
      float v[4], n2[2];
#pragma unroll
      for (int e = 0; e < 4; ++e) v[e] = tid < 448 ? __uint_as_float(y0[e]) : 0.f;
#pragma unroll
      for (int e = 0; e < 2; ++e) n2[e] = tred32(v[e], v[2 + e]);
      float n1 = tred16(n2[0], n2[1]);
      n1 += dpp<0x124>(n1); n1 += dpp<0x128>(n1);
      if (i16 < 4) *(LAS float*)(L + M_FIN + (wave * 16 + i16 * 4 + gq) * 4) = n1;
      BAR();
      if (tid < 16) {
        const int m = as * 16 + tid;
        float y = 0.f;
#pragma unroll
        for (int w = 0; w < 8; ++w) y += *(const LAS float*)(L + M_FIN + (w * 16 + tid) * 4);
        const float rsv = *(const LAS float*)(L + M_RS + ab * 4);
        const float xnv = bf2f(*(const LAS u16*)(L + L_XN + ab * XS + m * 2));
        y = rbf(rbf(y) * rsv);
        const float z = rbf(xnv + y);
        gate = rbf(z / (1.0f + __expf(-z)));
      }
    }
    {
      const u32 s5 = WS_IN5 + kb * 8192 + tid * 16;
      v4u a0 = sv;
      _Pragma("unroll 1") for (u32 spins = 0; spins < SPIN_CAP; ++spins) {
        CFENCE();
        if (umax4(a0) == SENT) a0 = __builtin_amdgcn_raw_buffer_load_b128(rs, s5, 0, 16);
        if (__all(umax4(a0) != SENT)) break;
        __builtin_amdgcn_s_sleep(1);
      }
      float n2[2];
#pragma unroll
      for (int e = 0; e < 2; ++e) n2[e] = tred32(__uint_as_float(a0[e]), __uint_as_float(a0[2 + e]));
      float n1 = tred16(n2[0], n2[1]);
      n1 += dpp<0x124>(n1); n1 += dpp<0x128>(n1);
      if (i16 < 4) *(LAS float*)(L + M_FIN + 512 + (wave * 16 + i16 * 4 + gq) * 4) = n1;
    }
    BAR();
    if (tid < 16) {
      float sacc = 0.f;
#pragma unroll
      for (int w = 0; w < 8; ++w) sacc += *(const LAS float*)(L + M_FIN + 512 + (w * 16 + tid) * 4);
      const float o = bf2f(xfin) + rbf(rbf(sacc) * gate);
      out[ab * 512 + as * 16 + tid] = __builtin_bit_cast(u16, (__bf16)o);
    }
    if (g == 0 && tid == 0) __builtin_amdgcn_raw_buffer_store_b64(v2u{tagA, tagB}, rs, WS_EPOCH, 0, 16);
  }
}

extern "C" void kernel_launch(void* const* d_in, const int* in_sizes, int n_in,
                              void* d_out, int out_size, void* d_ws, size_t ws_size,
                              hipStream_t stream) {
  (void)in_sizes; (void)n_in; (void)out_size; (void)ws_size;
  (void)hipFuncSetAttribute((const void*)mega, hipFuncAttributeMaxDynamicSharedMemorySize, LDS_BYTES);
  mega<<<NWG, 512, LDS_BYTES, stream>>>((const u16*)d_in[0], (const u16*)d_in[1], (const u16*)d_in[2], (const u16*)d_in[3],
                                        (const u16*)d_in[4], (const u16*)d_in[5], (const u16*)d_in[7], (const u16*)d_in[8],
                                        (const u16*)d_in[9], (const u16*)d_in[10], (u16*)d_out, (unsigned char*)d_ws);
}
```
